```python
import torch

def _softmax(x, axis=-1):
    return torch.softmax(x, dim=axis)
def _gelu(x):
    return torch.nn.functional.gelu(x, approximate="tanh")
def _silu(x):
    return torch.nn.functional.silu(x)
def _sigmoid(x):
    return torch.sigmoid(x)
def _layernorm(x, axis=-1, eps=1e-5):
    m = x.mean(dim=axis, keepdim=True)
    v = x.var(dim=axis, keepdim=True, unbiased=False)
    return (x - m) / torch.sqrt(v + eps)
def _scatter_add(tgt, idx, src, axis=0):
    return tgt.clone().index_add_(axis, idx, src)
def _segment_sum(src, seg, n):
    out = torch.zeros((n,) + src.shape[1:], dtype=src.dtype)
    return out.index_add_(0, seg, src)
def _pad(x, pw):
    flat = tuple(v for pair in reversed(pw) for v in pair)
    return torch.nn.functional.pad(x, flat)

kernel_name = 'recurrent_scan__gqa_block_cached__glu_gated_geglu__decode'

def setup_inputs():

    _w = (lambda a: (lambda t: t.to(torch.bfloat16) if t.is_floating_point() else t)(torch.as_tensor(a)))
    def _A_setup():
        B, Sq, D = 1, 1, 512
        Hq, Hkv, Sk, Dh = 32, 8, 32768, 128
        assert Hq % Hkv == 0, (Hq, Hkv)
        return {
            "x": _w((lambda *s: torch.randn(*s, dtype=torch.bfloat16) * 0.4)(B, Sq, D)),
            "k_cache": _w((lambda *s: torch.randn(*s, dtype=torch.bfloat16) * 1.6)(B, Hkv, Sk, Dh)),
            "v_cache": _w((lambda *s: torch.randn(*s, dtype=torch.bfloat16) * 1.6)(B, Hkv, Sk, Dh)),
            "wq": _w((lambda *s: torch.randn(*s, dtype=torch.bfloat16) * 1.6)(D, Hq * Dh) * (D ** -0.5)),
            "wo": _w((lambda *s: torch.randn(*s, dtype=torch.bfloat16) * 1.6)(Hq * Dh, D) * ((Hq * Dh) ** -0.5)),
        }
    _A_in = _A_setup()

    def _B_setup():
        B, S, Dm, Df = 1, 1, 512, 1792
        return {
            "x": _w((lambda *s: torch.randn(*s, dtype=torch.bfloat16) * 0.4)(B, S, Dm)),
            "w_gate": _w((lambda *s: torch.randn(*s, dtype=torch.bfloat16) * 1.6)(Dm, Df) * (Dm ** -0.5)),
            "w_up": _w((lambda *s: torch.randn(*s, dtype=torch.bfloat16) * 1.6)(Dm, Df) * (Dm ** -0.5)),
            "w_down": _w((lambda *s: torch.randn(*s, dtype=torch.bfloat16) * 1.6)(Df, Dm) * (Df ** -0.5)),
        }
    _B_in = _B_setup()

    return {
        "A_x": _A_in["x"],
        "A_k_cache": _A_in["k_cache"],
        "A_v_cache": _A_in["v_cache"],
        "A_wq": _A_in["wq"],
        "A_wo": _A_in["wo"],
        "B_x": _B_in["x"],
        "B_w_gate": _B_in["w_gate"],
        "B_w_up": _B_in["w_up"],
        "B_w_down": _B_in["w_down"],
    }

def reference(A_x, A_k_cache, A_v_cache, A_wq, A_wo, B_x, B_w_gate, B_w_up, B_w_down):
    def _A_ref(x, k_cache, v_cache, wq, wo):
        B, Sq, D = x.shape
        Hq, Dh = 32, 128
        Hkv = k_cache.shape[1]
        rep = Hq // Hkv
        q = (lambda x, *d: x.permute(*d))((x @ wq).reshape(B, Sq, Hq, Dh), 0, 2, 1, 3)
        k = (lambda x, n, axis: torch.repeat_interleave(x, n, dim=axis))(k_cache, rep, axis=1)
        v = (lambda x, n, axis: torch.repeat_interleave(x, n, dim=axis))(v_cache, rep, axis=1)
        scores = torch.einsum("bhqd,bhkd->bhqk", q, k) * (Dh ** -0.5)
        p = _softmax(scores, axis=-1)
        o = torch.einsum("bhqk,bhkd->bhqd", p, v)
        return (lambda x, *d: x.permute(*d))(o, 0, 2, 1, 3).reshape(B, Sq, Hq * Dh) @ wo

    def _B_ref(x, w_gate, w_up, w_down):
        g = torch.einsum("bsd,df->bsf", x, w_gate)
        u = torch.einsum("bsd,df->bsf", x, w_up)
        h = _gelu(g) * u
        return torch.einsum("bsf,fd->bsd", h, w_down)

    x = A_x
    xn = _layernorm(x, axis=-1)
    s = _A_ref(xn, A_k_cache, A_v_cache, A_wq, A_wo)
    g = _silu(_B_ref(xn, B_w_gate, B_w_up, B_w_down))
    return x + s * g
```

```cpp
#include <hip/hip_runtime.h>
#include <hip/hip_bf16.h>
#include <stdint.h>

#define GAS __attribute__((address_space(1)))
typedef unsigned v4u __attribute__((ext_vector_type(4)));
typedef unsigned v2u __attribute__((ext_vector_type(2)));
typedef float v4f __attribute__((ext_vector_type(4)));
typedef short v8s __attribute__((ext_vector_type(8)));
typedef short v4s __attribute__((ext_vector_type(4)));
typedef __bf16 v8bf __attribute__((ext_vector_type(8)));

constexpr int D = 512, HQ = 32, HKV = 8, SK = 32768, DH = 128, DF = 1792;

__device__ __forceinline__ float bf_lo(unsigned v) { return __uint_as_float(v << 16); }
__device__ __forceinline__ float bf_hi(unsigned v) { return __uint_as_float(v & 0xffff0000u); }
__device__ __forceinline__ unsigned cvt_pk_bf16(float lo, float hi) {
    unsigned r; asm("v_cvt_pk_bf16_f32 %0, %1, %2" : "=v"(r) : "v"(lo), "v"(hi)); return r;
}
__device__ __forceinline__ float rbf(float x) { return bf_lo(cvt_pk_bf16(x, x)); }
template <int CTRL> __device__ __forceinline__ float dppf(float v) {
    return __int_as_float(__builtin_amdgcn_update_dpp(0, __float_as_int(v), CTRL, 0xf, 0xf, true));
}
template <int CTRL> __device__ __forceinline__ unsigned dppu(unsigned v) {
    return (unsigned)__builtin_amdgcn_update_dpp(0, (int)v, CTRL, 0xf, 0xf, true);
}
__device__ __forceinline__ float wave_sum(float v) {
    for (int o = 32; o >= 1; o >>= 1) v += __shfl_xor(v, o);
    return v;
}

__device__ __forceinline__ void layernorm8(v4u xv, float (&xn)[8]) {
    float x[8];
    for (int i = 0; i < 4; ++i) { x[2 * i] = bf_lo(xv[i]); x[2 * i + 1] = bf_hi(xv[i]); }
    float s = 0; for (int i = 0; i < 8; ++i) s += x[i];
    s = wave_sum(s);
    const float mean = s * (1.f / D);
    float q = 0; for (int i = 0; i < 8; ++i) { float d = x[i] - mean; q += d * d; }
    q = wave_sum(q);
    const float mb = rbf(mean), vb = rbf(q * (1.f / D));
    const float sd = rbf(sqrtf(rbf(vb + 1e-5f)));
    for (int i = 0; i < 8; ++i) xn[i] = rbf(rbf(x[i] - mb) / sd);
}

__global__ void __launch_bounds__(256) k_pre(const unsigned short* __restrict__ x, const unsigned short* __restrict__ wq,
                                             const unsigned short* __restrict__ wg, const unsigned short* __restrict__ wu,
                                             unsigned short* __restrict__ qout, float* __restrict__ hout) {
    __shared__ float sxn[D];
    __shared__ float red[4][16];
    const int b = blockIdx.x, t = threadIdx.x, lane = t & 63, w = t >> 6;
    if (w == 0) { v4u xv = *(const v4u*)(x + 8 * lane); float xn[8]; layernorm8(xv, xn); for (int i = 0; i < 8; ++i) sxn[8 * lane + i] = xn[i]; }
    __syncthreads();
    float acc[8] = {0, 0, 0, 0, 0, 0, 0, 0};
    const int half = t & 1;
    const unsigned short* base; int ld;
    if (b < 256) { base = wq + 16 * b + 8 * half; ld = HQ * DH; }
    else { base = (half ? wu : wg) + 8 * (b - 256); ld = DF; }
    for (int r = t >> 1; r < D; r += 128) {
        v4u v = *(const v4u*)(base + (size_t)r * ld); float xr = sxn[r];
        for (int i = 0; i < 4; ++i) { acc[2 * i] += xr * bf_lo(v[i]); acc[2 * i + 1] += xr * bf_hi(v[i]); }
    }
    for (int i = 0; i < 8; ++i) for (int o = 32; o >= 2; o >>= 1) acc[i] += __shfl_xor(acc[i], o);
    if (lane < 2) for (int i = 0; i < 8; ++i) red[w][8 * lane + i] = acc[i];
    __syncthreads();
    if (b < 256) {
        if (t < 16) { float v = red[0][t] + red[1][t] + red[2][t] + red[3][t]; qout[16 * b + t] = (unsigned short)(cvt_pk_bf16(v, v) & 0xffff); }
    } else if (t < 8) {
        float g = rbf(red[0][t] + red[1][t] + red[2][t] + red[3][t]);
        float u = rbf(red[0][t + 8] + red[1][t + 8] + red[2][t + 8] + red[3][t + 8]);
        float ge = rbf(0.5f * g * (1.f + tanhf(0.7978845608028654f * (g + 0.044715f * g * g * g))));
        hout[8 * (b - 256) + t] = rbf(ge * u);
    }
}

constexpr int NW = 8;
constexpr int CH = 32;
constexpr int WIN = 24;
constexpr float CSC = 0.08838834764831845f * 1.4426950408889634f;

__global__ void __launch_bounds__(512, 2) k_attn(const unsigned short* __restrict__ kc, const unsigned short* __restrict__ vc,
                                                 const unsigned short* __restrict__ q, float* __restrict__ pml, float* __restrict__ pacc) {
    __shared__ float sm[NW][4], sl[NW][4];
    __shared__ float sacc[NW][512];
    const int b = blockIdx.x, g = b & 7, sp = b >> 3;
    const int t = threadIdx.x, lane = t & 63, w = __builtin_amdgcn_readfirstlane(t >> 6);
    const size_t base = ((size_t)g * SK + sp * 1024 + w * 4) * DH * 2;
    const GAS char* kp = (const GAS char*)kc + base + ((lane >> 2) & 3) * 256 + (lane >> 4) * 64 + (lane & 3) * 16;
    const GAS char* vp = (const GAS char*)vc + base + lane * 16;
    v4u qb = *(const v4u*)(q + (4 * g + ((lane >> 2) & 3)) * DH + 32 * (lane >> 4) + 8 * (lane & 3));
    __builtin_amdgcn_sched_barrier(0);
    v4u win[WIN];
#pragma unroll
    for (int n = 0; n < WIN; ++n) win[n] = __builtin_nontemporal_load((const GAS v4u*)(kp + n * 8192));
    __builtin_amdgcn_sched_barrier(0);
    const int qp = lane & 3;
    float S[8];
#pragma unroll
    for (int gi = 0; gi < 8; ++gi) {
        float e[4];
#pragma unroll
        for (int c = 0; c < 4; ++c) {
            const int n = 4 * gi + c;
            v4f d = __builtin_amdgcn_mfma_f32_16x16x32_bf16(__builtin_bit_cast(v8bf, win[n % WIN]), __builtin_bit_cast(v8bf, qb), (v4f){0, 0, 0, 0}, 0, 0, 0);
            const int m = n + WIN;
            if (m < CH) win[m % WIN] = __builtin_nontemporal_load((const GAS v4u*)(kp + m * 8192));
            else win[m % WIN] = __builtin_nontemporal_load((const GAS v4u*)(vp + (m - CH) * 8192));
            e[c] = qp == 0 ? d[0] : qp == 1 ? d[1] : qp == 2 ? d[2] : d[3];
        }
        const bool o1 = qp & 1, o2 = qp & 2;
        float k01 = (o1 ? e[1] : e[0]) + dppf<0xB1>(o1 ? e[0] : e[1]);
        float k23 = (o1 ? e[3] : e[2]) + dppf<0xB1>(o1 ? e[2] : e[3]);
        S[gi] = (o2 ? k23 : k01) + dppf<0x4E>(o2 ? k01 : k23);
        __builtin_amdgcn_sched_barrier(0);
    }
    float m = S[0];
#pragma unroll
    for (int i = 1; i < 8; ++i) m = fmaxf(m, S[i]);
    m = fmaxf(m, dppf<0xB1>(m)); m = fmaxf(m, dppf<0x4E>(m));
    m = fmaxf(m, __shfl_xor(m, 16)); m = fmaxf(m, __shfl_xor(m, 32));
    const float mc = m * CSC;
    float l = 0; unsigned pp[8];
#pragma unroll
    for (int i = 0; i < 8; ++i) { float p = __builtin_amdgcn_exp2f(S[i] * CSC - mc); l += p; pp[i] = cvt_pk_bf16(p, p); }
    l += dppf<0xB1>(l); l += dppf<0x4E>(l); l += __shfl_xor(l, 16); l += __shfl_xor(l, 32);
    if (lane < 16 && qp == 0) { sm[w][lane >> 2] = mc; sl[w][lane >> 2] = l; }
    const unsigned mk0 = qp == 0 ? 0xffffu : qp == 1 ? 0xffff0000u : 0u;
    const unsigned mk1 = qp == 2 ? 0xffffu : qp == 3 ? 0xffff0000u : 0u;
    v4f acc0 = {0, 0, 0, 0}, acc1 = {0, 0, 0, 0};
#pragma unroll
    for (int n = 0; n < CH; ++n) {
        unsigned pb = (n & 3) == 0 ? dppu<0x00>(pp[n >> 2]) : (n & 3) == 1 ? dppu<0x55>(pp[n >> 2]) : (n & 3) == 2 ? dppu<0xAA>(pp[n >> 2]) : dppu<0xFF>(pp[n >> 2]);
        v2u a = {pb & mk0, pb & mk1};
        v4u v = win[(n + CH) % WIN];
        v2u vlo = {v[0], v[1]}, vhi = {v[2], v[3]};
        acc0 = __builtin_amdgcn_mfma_f32_16x16x16bf16_1k(__builtin_bit_cast(v4s, a), __builtin_bit_cast(v4s, vlo), acc0, 0, 0, 0);
        acc1 = __builtin_amdgcn_mfma_f32_16x16x16bf16_1k(__builtin_bit_cast(v4s, a), __builtin_bit_cast(v4s, vhi), acc1, 0, 0, 0);
        const int mm = n + CH + WIN;
        if (mm < 2 * CH) win[mm % WIN] = __builtin_nontemporal_load((const GAS v4u*)(vp + (mm - CH) * 8192));
        __builtin_amdgcn_sched_barrier(0);
    }
    __syncthreads();
    {
        const int h = lane >> 4;
        float M = sm[0][h];
        for (int i = 1; i < NW; ++i) M = fmaxf(M, sm[i][h]);
        const float f = __builtin_amdgcn_exp2f(sm[w][h] - M);
        float* dst = &sacc[w][h * 128 + 8 * (lane & 15)];
        *(v4f*)dst = acc0 * f; *(v4f*)(dst + 4) = acc1 * f;
        if (t < 4) {
            float MM = sm[0][t]; for (int i = 1; i < NW; ++i) MM = fmaxf(MM, sm[i][t]);
            float L = 0; for (int i = 0; i < NW; ++i) L += sl[i][t] * __builtin_amdgcn_exp2f(sm[i][t] - MM);
            pml[(b * 4 + t) * 2] = MM; pml[(b * 4 + t) * 2 + 1] = L;
        }
    }
    __syncthreads();
    float s = 0;
    for (int i = 0; i < NW; ++i) s += sacc[i][t];
    pacc[b * 512 + t] = s;
}

__global__ void __launch_bounds__(256) k_comb(const float* __restrict__ pml, const float* __restrict__ pacc, const unsigned short* __restrict__ wo,
                                              const float* __restrict__ hin, const unsigned short* __restrict__ wd,
                                              float* __restrict__ spart, float* __restrict__ dpart) {
    __shared__ float so[16];
    const int b = blockIdx.x, g = b & 7, j = b >> 3, t = threadIdx.x;
    if (t < 16) {
        const int r = 16 * j + t, h = r >> 7;
        float M = -INFINITY;
        for (int i = 0; i < 32; ++i) M = fmaxf(M, pml[((i * 8 + g) * 4 + h) * 2]);
        float L = 0, o = 0;
        for (int i = 0; i < 32; ++i) {
            float f = __builtin_amdgcn_exp2f(pml[((i * 8 + g) * 4 + h) * 2] - M);
            L += f * pml[((i * 8 + g) * 4 + h) * 2 + 1]; o += f * pacc[(i * 8 + g) * 512 + r];
        }
        so[t] = rbf(o / L);
    }
    __syncthreads();
    float a0 = 0, a1 = 0;
    for (int r = 0; r < 16; ++r) { unsigned v = *(const unsigned*)(wo + (size_t)(512 * g + 16 * j + r) * D + 2 * t); a0 += so[r] * bf_lo(v); a1 += so[r] * bf_hi(v); }
    spart[b * 512 + 2 * t] = a0; spart[b * 512 + 2 * t + 1] = a1;
    if (b < 224) {
        float d0 = 0, d1 = 0;
        for (int r = 0; r < 8; ++r) { unsigned v = *(const unsigned*)(wd + (size_t)(8 * b + r) * D + 2 * t); float hv = hin[8 * b + r]; d0 += hv * bf_lo(v); d1 += hv * bf_hi(v); }
        dpart[b * 512 + 2 * t] = d0; dpart[b * 512 + 2 * t + 1] = d1;
    }
}

__global__ void __launch_bounds__(64) k_fin(const unsigned short* __restrict__ x, const float* __restrict__ spart, const float* __restrict__ dpart, unsigned short* __restrict__ out) {
    const int c = blockIdx.x, lane = threadIdx.x;
    float s = 0, d = 0;
    for (int i = lane; i < 256; i += 64) s += spart[i * 512 + c];
    for (int i = lane; i < 224; i += 64) d += dpart[i * 512 + c];
    s = wave_sum(s); d = wave_sum(d);
    if (lane == 0) {
        s = rbf(s); d = rbf(d);
        float gg = rbf(d / (1.f + __expf(-d)));
        float o = bf_lo(x[c]) + rbf(s * gg);
        out[c] = (unsigned short)(cvt_pk_bf16(o, o) & 0xffff);
    }
}

extern "C" void kernel_launch(void* const* d_in, const int* in_sizes, int n_in, void* d_out, int out_size, void* d_ws, size_t ws_size, hipStream_t stream) {
    const unsigned short* x = (const unsigned short*)d_in[0];
    const unsigned short* kc = (const unsigned short*)d_in[1];
    const unsigned short* vc = (const unsigned short*)d_in[2];
    const unsigned short* wq = (const unsigned short*)d_in[3];
    const unsigned short* wo = (const unsigned short*)d_in[4];
    const unsigned short* wg = (const unsigned short*)d_in[6];
    const unsigned short* wu = (const unsigned short*)d_in[7];
    const unsigned short* wd = (const unsigned short*)d_in[8];
    char* ws = (char*)d_ws;
    unsigned short* q = (unsigned short*)ws;
    float* h = (float*)(ws + 8192);
    float* pml = (float*)(ws + 16384);
    float* pacc = (float*)(ws + 32768);
    float* spart = pacc + 256 * 512;
    float* dpart = spart + 256 * 512;
    k_pre<<<256 + 224, 256, 0, stream>>>(x, wq, wg, wu, q, h);
    k_attn<<<256, 512, 0, stream>>>(kc, vc, q, pml, pacc);
    k_comb<<<256, 256, 0, stream>>>(pml, pacc, wo, h, wd, spart, dpart);
    k_fin<<<512, 64, 0, stream>>>(x, spart, dpart, (unsigned short*)d_out);
}
```

```cpp
#include <hip/hip_runtime.h>
#include <stdint.h>

#define GAS __attribute__((address_space(1)))
typedef unsigned v4u __attribute__((ext_vector_type(4)));
typedef unsigned v2u __attribute__((ext_vector_type(2)));
typedef float v4f __attribute__((ext_vector_type(4)));
typedef float v2f __attribute__((ext_vector_type(2)));
typedef short v4s __attribute__((ext_vector_type(4)));
typedef __bf16 v8bf __attribute__((ext_vector_type(8)));
typedef const GAS char* gptr;

constexpr int D = 512, SK = 32768, DH = 128, DF = 1792, QN = 4096;
constexpr int NAUX = 4, NSTR = 8, NTHR = 64 * (NAUX + NSTR);
constexpr int CH = 32;
#define WIN 24
#define DEP 6
constexpr float SCALE = 0.08838834764831845f, LOG2E = 1.4426950408889634f;

constexpr unsigned OFF_E = 0;
constexpr unsigned OFF_Q = 4096;
constexpr unsigned OFF_H = OFF_Q + 16384;
constexpr unsigned OFF_D = OFF_H + 8192;
constexpr unsigned OFF_ML = OFF_D + 65536;
constexpr unsigned OFF_O = OFF_ML + 16384;
constexpr unsigned OFF_S = OFF_O + (1u << 20);
constexpr unsigned WS_USED = OFF_S + (512u << 10);
constexpr unsigned SPIN_CAP = 1u << 15;
#define STAMP(k) do { } while (0)

__device__ __forceinline__ float bf_lo(unsigned v) { return __uint_as_float(v << 16); }
__device__ __forceinline__ float bf_hi(unsigned v) { return __uint_as_float(v & 0xffff0000u); }
__device__ __forceinline__ unsigned cvt_pk_bf16(float lo, float hi) {
    unsigned r; asm("v_cvt_pk_bf16_f32 %0, %1, %2" : "=v"(r) : "v"(lo), "v"(hi)); return r;
}
__device__ __forceinline__ float rbf(float x) { return bf_hi(cvt_pk_bf16(x, x)); }
template <int CTRL> __device__ __forceinline__ float dppf(float v) {
    return __int_as_float(__builtin_amdgcn_update_dpp(0, __float_as_int(v), CTRL, 0xf, 0xf, true));
}
template <int CTRL> __device__ __forceinline__ unsigned dppu(unsigned v) {
    return (unsigned)__builtin_amdgcn_update_dpp(0, (int)v, CTRL, 0xf, 0xf, true);
}
constexpr int X1 = 0xB1, X2 = 0x4E, ROR4 = 0x124, ROR8 = 0x128, HMIR = 0x141;
__device__ __forceinline__ float rs16(float a, float b) {
    auto r = __builtin_amdgcn_permlane16_swap(__float_as_uint(a), __float_as_uint(b), false, false);
    return __uint_as_float(r[0]) + __uint_as_float(r[1]);
}
__device__ __forceinline__ float rs32(float a, float b) {
    auto r = __builtin_amdgcn_permlane32_swap(__float_as_uint(a), __float_as_uint(b), false, false);
    return __uint_as_float(r[0]) + __uint_as_float(r[1]);
}
__device__ __forceinline__ float mx16(float a) {
    auto r = __builtin_amdgcn_permlane16_swap(__float_as_uint(a), __float_as_uint(a), false, false);
    return fmaxf(__uint_as_float(r[0]), __uint_as_float(r[1]));
}
__device__ __forceinline__ float mx32(float a) {
    auto r = __builtin_amdgcn_permlane32_swap(__float_as_uint(a), __float_as_uint(a), false, false);
    return fmaxf(__uint_as_float(r[0]), __uint_as_float(r[1]));
}
__device__ __forceinline__ float wave_sum(float v) {
    v += dppf<X1>(v); v += dppf<X2>(v); v += dppf<ROR4>(v); v += dppf<ROR8>(v);
    v = rs16(v, v); return rs32(v, v);
}
template <int N> __device__ __forceinline__ void wait_vm() { asm volatile("s_waitcnt vmcnt(%0)" :: "n"(N) : "memory"); }
__device__ __forceinline__ void cbar() { asm volatile("" ::: "memory"); }
__device__ __forceinline__ void wg_barrier() { asm volatile("s_waitcnt lgkmcnt(0)\n\ts_barrier" ::: "memory"); }
__device__ __forceinline__ v4u ldnt(gptr p) { return __builtin_nontemporal_load((const GAS v4u*)p); }
__device__ __forceinline__ void lds_signal(unsigned* w) {
    asm volatile("s_waitcnt lgkmcnt(0)" ::: "memory");
    if ((threadIdx.x & 63) == 0) __hip_atomic_fetch_add(w, 1u, __ATOMIC_RELAXED, __HIP_MEMORY_SCOPE_WORKGROUP);
    cbar();
}
__device__ __forceinline__ void lds_wait(unsigned* w, unsigned n) {
    cbar();
    while ((unsigned)__builtin_amdgcn_readfirstlane(__hip_atomic_load(w, __ATOMIC_RELAXED, __HIP_MEMORY_SCOPE_WORKGROUP)) < n) __builtin_amdgcn_s_sleep(1);
    cbar();
}
template <int V> struct IC { static constexpr int value = V; };
template <int I, int N, class F> __device__ __forceinline__ void sfor(F&& f) { if constexpr (I < N) { f(IC<I>{}); sfor<I + 1, N>(f); } }
constexpr int cmax(int a, int b) { return a > b ? a : b; }
typedef __amdgpu_buffer_rsrc_t rsrc_t;
__device__ __forceinline__ void put(rsrc_t rs, unsigned off, unsigned v0, unsigned v1, unsigned ta, unsigned tb) {
    __builtin_amdgcn_raw_buffer_store_b128((v4u){v0, ta, v1, tb}, rs, off, 0, 16);
}
__device__ __forceinline__ void gather1(v4u rd, unsigned o0, v4u& g0, unsigned ta, unsigned tb) {
    bool ok = false;
    g0 = (v4u){0, 0, 0, 0};
#pragma nounroll
    for (unsigned spins = 0; spins < SPIN_CAP; ++spins) {
        if (!ok) {
            asm volatile("buffer_load_dwordx4 %0, %1, %2, 0 offen sc1\n\ts_waitcnt vmcnt(0)" : "+v"(g0) : "v"(o0), "s"(rd) : "memory");
            ok = (g0[1] == ta) & (g0[3] == tb);
        }
        if (__all(ok)) break;
    }
}
__device__ __forceinline__ void gather2(v4u rd, unsigned o0, unsigned o1, v4u& g0, v4u& g1, unsigned ta, unsigned tb) {
    bool ok = false;
    g0 = (v4u){0, 0, 0, 0}; g1 = g0;
#pragma nounroll
    for (unsigned spins = 0; spins < SPIN_CAP; ++spins) {
        if (!ok) {
            asm volatile("buffer_load_dwordx4 %0, %2, %4, 0 offen sc1\n\tbuffer_load_dwordx4 %1, %3, %4, 0 offen sc1\n\ts_waitcnt vmcnt(0)"
                         : "+v"(g0), "+v"(g1) : "v"(o0), "v"(o1), "s"(rd) : "memory");
            ok = (g0[1] == ta) & (g0[3] == tb) & (g1[1] == ta) & (g1[3] == tb);
        }
        if (__all(ok)) break;
    }
}
__device__ __forceinline__ float gemv16(const v4u (&w)[4], const float (&xr)[4], int lane) {
    float a[8] = {0, 0, 0, 0, 0, 0, 0, 0};
#pragma unroll
    for (int k = 0; k < 4; ++k)
#pragma unroll
        for (int i = 0; i < 4; ++i) { a[2 * i] = fmaf(xr[k], bf_lo(w[k][i]), a[2 * i]); a[2 * i + 1] = fmaf(xr[k], bf_hi(w[k][i]), a[2 * i + 1]); }
    const bool b1 = lane & 2;
    float t[4];
#pragma unroll
    for (int i = 0; i < 4; ++i) {
        t[i] = (b1 ? a[4 + i] : a[i]) + dppf<X2>(b1 ? a[i] : a[4 + i]);
        t[i] += dppf<ROR4>(t[i]); t[i] += dppf<ROR8>(t[i]);
    }
    return rs32(rs16(t[0], t[1]), rs16(t[2], t[3]));
}

struct __attribute__((aligned(16))) Lds {
    float sacc[NSTR][576];
    float xn[NAUX][D];
    float ss[NAUX][256];
    float redq[NAUX][16], redf[NAUX][16];
    unsigned q[256];
    unsigned h[64];
    float sm[NSTR][4], sl[NSTR][4];
    float gm[4], gi[4];
    unsigned tag[2];
    unsigned cq, cf, cml, fq, fml;
};

__global__ void __launch_bounds__(NTHR, 3)
fused_step(const unsigned short* __restrict__ x, const unsigned short* __restrict__ kc, const unsigned short* __restrict__ vc,
           const unsigned short* __restrict__ wq, const unsigned short* __restrict__ wo, const unsigned short* __restrict__ wg,
           const unsigned short* __restrict__ wu, const unsigned short* __restrict__ wd, unsigned short* __restrict__ out, char* ws) {
    __shared__ Lds L;
    const int b = blockIdx.x, lane = threadIdx.x & 63, wv = __builtin_amdgcn_readfirstlane(threadIdx.x >> 6);
    const int xcd = b & 7, r = b >> 3;
    const int g = r & 7, sp = (r >> 3) * 8 + xcd;
    const rsrc_t rs = __builtin_amdgcn_make_buffer_rsrc(ws, 0, WS_USED, 0x00020000);
    const v4u rd = {(unsigned)(uintptr_t)ws, (unsigned)((uintptr_t)ws >> 32) & 0xffffu, WS_USED, 0x00020000u};

    if (wv >= NAUX) {
        const int w = wv - NAUX;
        const size_t base = ((size_t)g * SK + sp * 1024 + w * 4) * (DH * 2);
        gptr kp = (gptr)kc + base + ((lane >> 2) & 3) * 256 + (lane >> 4) * 64 + (lane & 3) * 16;
        gptr vp = (gptr)vc + base + lane * 16;
        const int qp = lane & 3;
        const bool o1 = qp & 1, o2 = qp & 2;
        const unsigned mk0 = qp == 0 ? 0xffffu : qp == 1 ? 0xffff0000u : 0u;
        const unsigned mk1 = qp == 2 ? 0xffffu : qp == 3 ? 0xffff0000u : 0u;
        v4u win[WIN]; v4u qb; float S[8]; unsigned pp[8];
        v4f acc0 = {0, 0, 0, 0}, acc1 = {0, 0, 0, 0};
        wg_barrier();
        constexpr int MSOFT = cmax(CH - 1 + DEP, WIN + 7);
        sfor<0, 2 * CH + DEP + 1>([&](auto mi) __attribute__((always_inline)) {
            constexpr int m = decltype(mi)::value;
            if constexpr (m >= DEP && m < 2 * CH) wait_vm<DEP - 1>();
            if constexpr (m == WIN) {
                if (w == 0) STAMP(3);
                lds_wait(&L.fq, 1);
                qb = *(const v4u*)&L.q[((lane >> 2) & 3) * 64 + 16 * (lane >> 4) + 4 * qp];
            }
            sfor<0, 8>([&](auto gii) __attribute__((always_inline)) {
                constexpr int gi = decltype(gii)::value;
                if constexpr (m == cmax(4 * gi + 3 + DEP, WIN + gi)) {
                    float e[4];
#pragma unroll
                    for (int c = 0; c < 4; ++c) {
                        v4f d = __builtin_amdgcn_mfma_f32_16x16x32_bf16(__builtin_bit_cast(v8bf, win[(4 * gi + c) % WIN]), __builtin_bit_cast(v8bf, qb), (v4f){0, 0, 0, 0}, 0, 0, 0);
                        e[c] = o2 ? (o1 ? d[3] : d[2]) : (o1 ? d[1] : d[0]);
                    }
                    const float k01 = (o1 ? e[1] : e[0]) + dppf<X1>(o1 ? e[0] : e[1]);
                    const float k23 = (o1 ? e[3] : e[2]) + dppf<X1>(o1 ? e[2] : e[3]);
                    S[gi] = rbf(rbf((o2 ? k23 : k01) + dppf<X2>(o2 ? k01 : k23)) * SCALE);
                }
            });
            if constexpr (m == MSOFT) {
                float mx = S[0];
#pragma unroll
                for (int i = 1; i < 8; ++i) mx = fmaxf(mx, S[i]);
                mx = fmaxf(mx, dppf<X1>(mx)); mx = fmaxf(mx, dppf<X2>(mx)); mx = mx32(mx16(mx));
                const float mc = mx * LOG2E;
                float l = 0;
#pragma unroll
                for (int i = 0; i < 8; ++i) { const float p = __builtin_amdgcn_exp2f(fmaf(S[i], LOG2E, -mc)); l += p; pp[i] = cvt_pk_bf16(p, p); }
                l += dppf<X1>(l); l += dppf<X2>(l); l = rs16(l, l); l = rs32(l, l);
                if (lane < 16 && qp == 0) { L.sm[w][lane >> 2] = mc; L.sl[w][lane >> 2] = l; }
                lds_signal(&L.cml);
                if (w == 0) STAMP(4);
            }
            sfor<0, CH>([&](auto ni) __attribute__((always_inline)) {
                constexpr int n = decltype(ni)::value;
                if constexpr (m == cmax(CH + n + DEP, MSOFT + 1)) {
                    const unsigned pb = dppu<(n & 3) * 0x55>(pp[n >> 2]);
                    const v2u a = {pb & mk0, pb & mk1};
                    const v4u v = win[(n + CH) % WIN];
                    const v2u vlo = {v[0], v[1]}, vhi = {v[2], v[3]};
                    acc0 = __builtin_amdgcn_mfma_f32_16x16x16bf16_1k(__builtin_bit_cast(v4s, a), __builtin_bit_cast(v4s, vlo), acc0, 0, 0, 0);
                    acc1 = __builtin_amdgcn_mfma_f32_16x16x16bf16_1k(__builtin_bit_cast(v4s, a), __builtin_bit_cast(v4s, vhi), acc1, 0, 0, 0);
                }
            });
            __builtin_amdgcn_sched_barrier(0);
            if constexpr (m < CH) win[m % WIN] = ldnt(kp + m * (NSTR * 1024));
            else if constexpr (m < 2 * CH) win[m % WIN] = ldnt(vp + (m - CH) * (NSTR * 1024));
            if constexpr (m == 2 * CH - 1) lds_wait(&L.fml, 1);
            __builtin_amdgcn_sched_barrier(0);
        });
        {
            const int h = lane >> 4;
            const float f = __builtin_amdgcn_exp2f(L.sm[w][h] - L.gm[h]) * L.gi[h];
            *(v4f*)&L.sacc[w][4 * lane] = acc0 * f;
            *(v4f*)&L.sacc[w][288 + 4 * lane] = acc1 * f;
        }
        if (w == 0) STAMP(6);
        wg_barrier();
        if (w < 4) {
            const int t = w * 64 + lane;
            const int idx = ((t >> 1) & 1) * 288 + (t >> 2) * 4 + (t & 1) * 2;
            v2f s = *(const v2f*)&L.sacc[0][idx];
#pragma unroll
            for (int i = 1; i < NSTR; ++i) s += *(const v2f*)&L.sacc[i][idx];
            put(rs, OFF_O + (g * 32 + sp) * 4096 + t * 16, __float_as_uint(s[0]), __float_as_uint(s[1]), L.tag[0], L.tag[1]);
            if (w == 0) STAMP(7);
        }
        return;
    }

    const int a = wv;
    if (a == 0) STAMP(0);
    const v2u ev = __builtin_amdgcn_raw_buffer_load_b64(rs, OFF_E, 0, 16);
    const v4u xv = *(const GAS v4u*)((gptr)x + lane * 16);
    const int qs = xcd * 32 + r;
    v4u wqv[4], wfv[4];
    {
        gptr p = (gptr)wq + (size_t)(128 * a + (lane >> 1)) * (QN * 2) + qs * 32 + (lane & 1) * 16;
#pragma unroll
        for (int k = 0; k < 4; ++k) wqv[k] = ldnt(p + k * (32 * QN * 2));
    }
    if (threadIdx.x == 0) { L.cq = 0; L.cf = 0; L.cml = 0; L.fq = 0; L.fml = 0; }
    __builtin_amdgcn_sched_barrier(0);
    wg_barrier();
    const bool ffn = r < 28;
    const int fs = xcd * 28 + (ffn ? r : 27);
    {
        gptr p = (gptr)((lane & 1) ? wu : wg) + (size_t)(128 * a + (lane >> 1)) * (DF * 2) + fs * 16;
#pragma unroll
        for (int k = 0; k < 4; ++k) wfv[k] = ldnt(p + k * (32 * DF * 2));
    }
    __builtin_amdgcn_sched_barrier(0);
    const unsigned ta = __builtin_amdgcn_readfirstlane(ev[0]) + 1u, tb = __builtin_amdgcn_readfirstlane(ev[1]) + 1u;
    float xr[4];
    {
        float xf[8];
#pragma unroll
        for (int i = 0; i < 4; ++i) { xf[2 * i] = bf_lo(xv[i]); xf[2 * i + 1] = bf_hi(xv[i]); }
        float s = 0;
#pragma unroll
        for (int i = 0; i < 8; ++i) s += xf[i];
        const float mean = wave_sum(s) * (1.f / D);
        float q2 = 0;
#pragma unroll
        for (int i = 0; i < 8; ++i) { const float d = xf[i] - mean; q2 = fmaf(d, d, q2); }
        const float var = wave_sum(q2) * (1.f / D);
        const float mb = rbf(mean), sd = rbf(sqrtf(rbf(rbf(var) + 1e-5f)));
        v4f n0, n1;
#pragma unroll
        for (int i = 0; i < 4; ++i) { n0[i] = rbf(rbf(xf[i] - mb) / sd); n1[i] = rbf(rbf(xf[4 + i] - mb) / sd); }
        *(v4f*)&L.xn[a][8 * lane] = n0; *(v4f*)&L.xn[a][8 * lane + 4] = n1;
        cbar();
#pragma unroll
        for (int k = 0; k < 4; ++k) xr[k] = L.xn[a][128 * a + 32 * k + (lane >> 1)];
    }
    const int rc = 8 * (lane & 1) + 4 * ((lane >> 1) & 1) + 2 * (lane >> 5) + ((lane >> 4) & 1);
    {
        const float v = gemv16(wqv, xr, lane);
        if ((lane & 12) == 0) L.redq[a][rc] = v;
        lds_signal(&L.cq);
    }
    if (a == 0) {
        lds_wait(&L.cq, NAUX);
        if (lane < 4) {
            const v4f s = *(const v4f*)&L.redq[0][4 * lane] + *(const v4f*)&L.redq[1][4 * lane] + *(const v4f*)&L.redq[2][4 * lane] + *(const v4f*)&L.redq[3][4 * lane];
            put(rs, OFF_Q + (qs * 4 + lane) * 16, cvt_pk_bf16(s[0], s[1]), cvt_pk_bf16(s[2], s[3]), ta, tb);
        }
        STAMP(1);
    }
    {
        const float v = gemv16(wfv, xr, lane);
        if ((lane & 12) == 0) L.redf[a][rc] = v;
        lds_signal(&L.cf);
    }
    const int rb = sp >> 1, chh = sp & 1;
    float gate2 = 0.f; unsigned x2 = 0;
    if (a == 0) {
        v4u gq[2]; const unsigned off[2] = {OFF_Q + (g * 128 + lane) * 16, OFF_Q + (g * 128 + 64 + lane) * 16};
        gather2(rd, off[0], off[1], gq[0], gq[1], ta, tb);
        *(v2u*)&L.q[2 * lane] = (v2u){gq[0][0], gq[0][2]};
        *(v2u*)&L.q[128 + 2 * lane] = (v2u){gq[1][0], gq[1][2]};
        if (lane == 0) { L.tag[0] = ta; L.tag[1] = tb; }
        lds_signal(&L.fq);
        STAMP(2);
    } else if (a == 1) {
        lds_wait(&L.cf, NAUX);
        if (lane < 2 && ffn) {
            const v4f gs = *(const v4f*)&L.redf[0][4 * lane] + *(const v4f*)&L.redf[1][4 * lane] + *(const v4f*)&L.redf[2][4 * lane] + *(const v4f*)&L.redf[3][4 * lane];
            const v4f us = *(const v4f*)&L.redf[0][8 + 4 * lane] + *(const v4f*)&L.redf[1][8 + 4 * lane] + *(const v4f*)&L.redf[2][8 + 4 * lane] + *(const v4f*)&L.redf[3][8 + 4 * lane];
            float hv[4];
#pragma unroll
            for (int i = 0; i < 4; ++i) {
                const float gg = rbf(gs[i]), uu = rbf(us[i]);
                const float th = 1.f - 2.f / (__expf(1.5957691216057308f * (gg + 0.044715f * gg * gg * gg)) + 1.f);
                hv[i] = rbf(0.5f * gg * (1.f + th)) * uu;
            }
            put(rs, OFF_H + (fs * 2 + lane) * 16, cvt_pk_bf16(hv[0], hv[1]), cvt_pk_bf16(hv[2], hv[3]), ta, tb);
        }
    } else if (a == 3) {
        const int drb = qs >> 4, dcb = qs & 15;
        v4u wdv[7];
        gptr p = (gptr)wd + (size_t)(112 * drb + (lane >> 2)) * (D * 2) + dcb * 64 + (lane & 3) * 16;
#pragma unroll
        for (int k = 0; k < 7; ++k) wdv[k] = ldnt(p + k * (16 * D * 2));
        x2 = *(const GAS unsigned*)((gptr)x + b * 4);
        v4u gh[1]; const unsigned off[1] = {OFF_H + (28 * drb + (lane < 28 ? lane : 27)) * 16};
        gather1(rd, off[0], gh[0], ta, tb);
        if (lane < 28) *(v2u*)&L.h[2 * lane] = (v2u){gh[0][0], gh[0][2]};
        cbar();
        float c[8] = {0, 0, 0, 0, 0, 0, 0, 0};
#pragma unroll
        for (int k = 0; k < 7; ++k) {
            const float hv = bf_lo(((const unsigned short*)L.h)[16 * k + (lane >> 2)]);
#pragma unroll
            for (int i = 0; i < 4; ++i) { c[2 * i] = fmaf(hv, bf_lo(wdv[k][i]), c[2 * i]); c[2 * i + 1] = fmaf(hv, bf_hi(wdv[k][i]), c[2 * i + 1]); }
        }
#pragma unroll
        for (int i = 0; i < 8; ++i) { c[i] += dppf<ROR4>(c[i]); c[i] += dppf<ROR8>(c[i]); }
        const float w0 = rs32(rs16(c[0], c[2]), rs16(c[4], c[6])), w1 = rs32(rs16(c[1], c[3]), rs16(c[5], c[7]));
        if ((lane & 12) == 0) put(rs, OFF_D + (qs * 16 + 4 * (lane & 3) + 2 * (lane >> 5) + ((lane >> 4) & 1)) * 16, __float_as_uint(w0), __float_as_uint(w1), ta, tb);
        v4u gd[1]; const unsigned off2[1] = {OFF_D + (((lane & 15) * 16 + (b >> 4)) * 16 + (b & 15)) * 16};
        gather1(rd, off2[0], gd[0], ta, tb);
        float d0 = __uint_as_float(gd[0][0]), d1 = __uint_as_float(gd[0][2]);
        d0 += dppf<X1>(d0); d0 += dppf<X2>(d0); d0 += dppf<ROR4>(d0); d0 += dppf<ROR8>(d0);
        d1 += dppf<X1>(d1); d1 += dppf<X2>(d1); d1 += dppf<ROR4>(d1); d1 += dppf<ROR8>(d1);
        const float dd = rbf(lane >= 32 ? d1 : d0);
        gate2 = rbf(dd / (1.f + __expf(-dd)));
        STAMP(12);
    }
    float wof[4][8];
    {
        gptr p = (gptr)wo + (size_t)(512 * g + 32 * rb + 8 * a + (lane >> 5)) * (D * 2) + chh * 512 + (lane & 31) * 16;
        v4u t[4];
#pragma unroll
        for (int k = 0; k < 4; ++k) t[k] = ldnt(p + k * (2 * D * 2));
#pragma unroll
        for (int k = 0; k < 4; ++k)
#pragma unroll
            for (int i = 0; i < 4; ++i) { wof[k][2 * i] = bf_lo(t[k][i]); wof[k][2 * i + 1] = bf_hi(t[k][i]); }
    }
    if (a == 2) {
        lds_wait(&L.cml, NSTR);
        const float m = L.sm[lane & 7][(lane >> 3) & 3], l = L.sl[lane & 7][(lane >> 3) & 3];
        float M = fmaxf(m, dppf<X1>(m)); M = fmaxf(M, dppf<X2>(M)); M = fmaxf(M, dppf<HMIR>(M));
        float s = l * __builtin_amdgcn_exp2f(m - M);
        s += dppf<X1>(s); s += dppf<X2>(s); s += dppf<HMIR>(s);
        if (lane < 32 && (lane & 7) == 0) put(rs, OFF_ML + ((g * 32 + sp) * 4 + (lane >> 3)) * 16, __float_as_uint(M), __float_as_uint(s), ta, tb);
        v4u gm[2]; const unsigned off[2] = {OFF_ML + (g * 128 + lane) * 16, OFF_ML + (g * 128 + 64 + lane) * 16};
        gather2(rd, off[0], off[1], gm[0], gm[1], ta, tb);
        const float m0 = __uint_as_float(gm[0][0]), m1 = __uint_as_float(gm[1][0]);
        float G = fmaxf(m0, m1);
        G = fmaxf(G, dppf<ROR4>(G)); G = fmaxf(G, dppf<ROR8>(G)); G = mx32(mx16(G));
        float T = __uint_as_float(gm[0][2]) * __builtin_amdgcn_exp2f(m0 - G) + __uint_as_float(gm[1][2]) * __builtin_amdgcn_exp2f(m1 - G);
        T += dppf<ROR4>(T); T += dppf<ROR8>(T); T = rs16(T, T); T = rs32(T, T);
        if (lane < 4) { L.gm[lane] = G; L.gi[lane] = 1.f / T; }
        lds_signal(&L.fml);
        STAMP(5);
    }
#pragma unroll
    for (int k = 0; k < 4; ++k)
#pragma unroll
        for (int i = 0; i < 8; ++i) asm volatile("" : "+v"(wof[k][i]));
    wg_barrier();
    {
        const unsigned o0 = OFF_O + (g * 32 + (lane >> 2)) * 4096 + (16 * rb + 4 * a + (lane & 3)) * 16;
        v4u go[2]; const unsigned off[2] = {o0, o0 + 16 * 4096};
        gather2(rd, off[0], off[1], go[0], go[1], ta, tb);
        if (a == 0) STAMP(8);
        float v0 = __uint_as_float(go[0][0]) + __uint_as_float(go[1][0]), v1 = __uint_as_float(go[0][2]) + __uint_as_float(go[1][2]);
        v0 += dppf<ROR4>(v0); v1 += dppf<ROR4>(v1); v0 += dppf<ROR8>(v0); v1 += dppf<ROR8>(v1);
        const float o = rbf(rs32(rs16(v0, v0), rs16(v1, v1)));
        const float ok[4] = {dppf<0x00>(o), dppf<0x55>(o), dppf<0xAA>(o), dppf<0xFF>(o)};
        float c[8];
#pragma unroll
        for (int i = 0; i < 8; ++i) c[i] = ok[0] * wof[0][i] + ok[1] * wof[1][i] + ok[2] * wof[2][i] + ok[3] * wof[3][i];
        v4f s4;
#pragma unroll
        for (int i = 0; i < 4; ++i) s4[i] = rs32(c[i], c[4 + i]);
        *(v4f*)&L.ss[a][8 * (lane & 31) + 4 * (lane >> 5)] = s4;
    }
    wg_barrier();
    if (a < 2) {
        const int n = 64 * a + lane;
        const v2f s = *(const v2f*)&L.ss[0][2 * n] + *(const v2f*)&L.ss[1][2 * n] + *(const v2f*)&L.ss[2][2 * n] + *(const v2f*)&L.ss[3][2 * n];
        put(rs, OFF_S + (chh * 128 + g * 16 + rb) * 2048 + n * 16, __float_as_uint(s[0]), __float_as_uint(s[1]), ta, tb);
        if (a == 0) STAMP(9);
    } else if (a == 3) {
        const unsigned o0 = OFF_S + ((b >> 7) * 128 + lane) * 2048 + (b & 127) * 16;
        v4u gs[2]; const unsigned off[2] = {o0, o0 + 64 * 2048};
        gather2(rd, off[0], off[1], gs[0], gs[1], ta, tb);
        STAMP(10);
        float v0 = __uint_as_float(gs[0][0]) + __uint_as_float(gs[1][0]), v1 = __uint_as_float(gs[0][2]) + __uint_as_float(gs[1][2]);
        v0 += dppf<X1>(v0); v1 += dppf<X1>(v1); v0 += dppf<X2>(v0); v1 += dppf<X2>(v1);
        v0 += dppf<ROR4>(v0); v1 += dppf<ROR4>(v1); v0 += dppf<ROR8>(v0); v1 += dppf<ROR8>(v1);
        const float s = rbf(rs32(rs16(v0, v0), rs16(v1, v1)));
        const float res = (lane >= 32 ? bf_hi(x2) : bf_lo(x2)) + rbf(s * gate2);
        if ((lane & 31) == 0) out[2 * b + (lane >> 5)] = (unsigned short)(cvt_pk_bf16(res, res) & 0xffffu);
        if (b == 0 && lane == 0) __builtin_amdgcn_raw_buffer_store_b64((v2u){ta, tb}, rs, OFF_E, 0, 16);
        STAMP(11);
    }
}

extern "C" void kernel_launch(void* const* d_in, const int* in_sizes, int n_in, void* d_out, int out_size, void* d_ws, size_t ws_size, hipStream_t stream) {
    fused_step<<<256, NTHR, 0, stream>>>((const unsigned short*)d_in[0], (const unsigned short*)d_in[1], (const unsigned short*)d_in[2],
                                         (const unsigned short*)d_in[3], (const unsigned short*)d_in[4], (const unsigned short*)d_in[6],
                                         (const unsigned short*)d_in[7], (const unsigned short*)d_in[8], (unsigned short*)d_out, (char*)d_ws);
}
```

```cpp
#include <hip/hip_runtime.h>
#include <stdint.h>

#define GAS __attribute__((address_space(1)))
typedef unsigned v4u __attribute__((ext_vector_type(4)));
typedef unsigned v2u __attribute__((ext_vector_type(2)));
typedef float v4f __attribute__((ext_vector_type(4)));
typedef float v2f __attribute__((ext_vector_type(2)));
typedef short v4s __attribute__((ext_vector_type(4)));
typedef __bf16 v8bf __attribute__((ext_vector_type(8)));
typedef const GAS char* gptr;

constexpr int D = 512, SK = 32768, DH = 128, DF = 1792, QN = 4096;
constexpr int NAUX = 4, NSTR = 4, NTHR = 64 * (NAUX + NSTR);
constexpr int CH = 64;
#define WIN 56
#define DEP0 16
#define TOKIL 1
constexpr int CSTR = TOKIL ? 32 * NSTR * 1024 : NSTR * 1024;
#define DEP 16
#define QSIG 40
#define SLP_Q 16
#define SLP_F 32
#define SLP_H 2
#define KV_AUX 0
#define W_NT 0
#define WO_IT 2
constexpr float SCALE = 0.08838834764831845f, LOG2E = 1.4426950408889634f;

constexpr unsigned OFF_E = 0;
constexpr unsigned OFF_Q = 4096;
constexpr unsigned OFF_H = OFF_Q + 16384;
constexpr unsigned OFF_D = OFF_H + 8192;
constexpr unsigned OFF_ML = OFF_D + 65536;
constexpr unsigned OFF_O = OFF_ML + 16384;
constexpr unsigned OFF_S = OFF_O + (1u << 20);
constexpr unsigned WS_USED = OFF_S + (512u << 10);
constexpr unsigned SPIN_CAP = 1u << 15;
#define STAMP(k) do { } while (0)
#define FLUSH(mask) do { } while (0)

__device__ __forceinline__ float bf_lo(unsigned v) { return __uint_as_float(v << 16); }
__device__ __forceinline__ float bf_hi(unsigned v) { return __uint_as_float(v & 0xffff0000u); }
__device__ __forceinline__ unsigned cvt_pk_bf16(float lo, float hi) {
    unsigned r; asm("v_cvt_pk_bf16_f32 %0, %1, %2" : "=v"(r) : "v"(lo), "v"(hi)); return r;
}
__device__ __forceinline__ float rbf(float x) { return bf_hi(cvt_pk_bf16(x, x)); }
template <int CTRL> __device__ __forceinline__ float dppf(float v) {
    return __int_as_float(__builtin_amdgcn_update_dpp(0, __float_as_int(v), CTRL, 0xf, 0xf, true));
}
template <int CTRL> __device__ __forceinline__ unsigned dppu(unsigned v) {
    return (unsigned)__builtin_amdgcn_update_dpp(0, (int)v, CTRL, 0xf, 0xf, true);
}
constexpr int X1 = 0xB1, X2 = 0x4E, ROR4 = 0x124, ROR8 = 0x128, HMIR = 0x141;
__device__ __forceinline__ float rs16(float a, float b) {
    auto r = __builtin_amdgcn_permlane16_swap(__float_as_uint(a), __float_as_uint(b), false, false);
    return __uint_as_float(r[0]) + __uint_as_float(r[1]);
}
__device__ __forceinline__ float rs32(float a, float b) {
    auto r = __builtin_amdgcn_permlane32_swap(__float_as_uint(a), __float_as_uint(b), false, false);
    return __uint_as_float(r[0]) + __uint_as_float(r[1]);
}
__device__ __forceinline__ float mx16(float a) {
    auto r = __builtin_amdgcn_permlane16_swap(__float_as_uint(a), __float_as_uint(a), false, false);
    return fmaxf(__uint_as_float(r[0]), __uint_as_float(r[1]));
}
__device__ __forceinline__ float mx32(float a) {
    auto r = __builtin_amdgcn_permlane32_swap(__float_as_uint(a), __float_as_uint(a), false, false);
    return fmaxf(__uint_as_float(r[0]), __uint_as_float(r[1]));
}
__device__ __forceinline__ float wave_sum(float v) {
    v += dppf<X1>(v); v += dppf<X2>(v); v += dppf<ROR4>(v); v += dppf<ROR8>(v);
    v = rs16(v, v); return rs32(v, v);
}
template <int N> __device__ __forceinline__ void wait_vm() { asm volatile("s_waitcnt vmcnt(%0)" :: "n"(N) : "memory"); }
__device__ __forceinline__ void cbar() { asm volatile("" ::: "memory"); }
__device__ __forceinline__ void wg_barrier() { asm volatile("s_waitcnt lgkmcnt(0)\n\ts_barrier" ::: "memory"); }
__device__ __forceinline__ v4u ldw(gptr p) {
    return W_NT ? __builtin_nontemporal_load((const GAS v4u*)p) : *(const GAS v4u*)p; }
__device__ __forceinline__ void lds_signal(unsigned* w) {
    asm volatile("s_waitcnt lgkmcnt(0)" ::: "memory");
    if ((threadIdx.x & 63) == 0) __hip_atomic_fetch_add(w, 1u, __ATOMIC_RELAXED, __HIP_MEMORY_SCOPE_WORKGROUP);
    cbar();
}
__device__ __forceinline__ void lds_wait(unsigned* w, unsigned n) {
    cbar();
    while ((unsigned)__builtin_amdgcn_readfirstlane(__hip_atomic_load(w, __ATOMIC_RELAXED, __HIP_MEMORY_SCOPE_WORKGROUP)) < n) __builtin_amdgcn_s_sleep(1);
    cbar();
}
template <int V> struct IC { static constexpr int value = V; };
template <int I, int N, class F> __device__ __forceinline__ void sfor(F&& f) { if constexpr (I < N) { f(IC<I>{}); sfor<I + 1, N>(f); } }
constexpr int cmax(int a, int b) { return a > b ? a : b; }
typedef __amdgpu_buffer_rsrc_t rsrc_t;
__device__ __forceinline__ void put(rsrc_t rs, unsigned off, unsigned v0, unsigned v1, unsigned ta, unsigned tb) {
    __builtin_amdgcn_raw_buffer_store_b128((v4u){v0, ta, v1, tb}, rs, off, 0, 16);
}
template <int SLP> __device__ __forceinline__ void gather1(v4u rd, unsigned o0, v4u& g0, unsigned ta, unsigned tb) {
    bool ok = false;
    g0 = (v4u){0, 0, 0, 0};
#pragma nounroll
    for (unsigned spins = 0; spins < SPIN_CAP; ++spins) {
        if (!ok) {
            asm volatile("buffer_load_dwordx4 %0, %1, %2, 0 offen sc1\n\ts_waitcnt vmcnt(0)" : "+v"(g0) : "v"(o0), "s"(rd) : "memory");
            ok = (g0[1] == ta) & (g0[3] == tb);
        }
        if (__all(ok)) break;
        if constexpr (SLP > 0) __builtin_amdgcn_s_sleep(SLP);
    }
}
template <int SLP> __device__ __forceinline__ void gather2(v4u rd, unsigned o0, unsigned o1, v4u& g0, v4u& g1, unsigned ta, unsigned tb) {
    bool ok = false;
    g0 = (v4u){0, 0, 0, 0}; g1 = g0;
#pragma nounroll
    for (unsigned spins = 0; spins < SPIN_CAP; ++spins) {
        if (!ok) {
            asm volatile("buffer_load_dwordx4 %0, %2, %4, 0 offen sc1\n\tbuffer_load_dwordx4 %1, %3, %4, 0 offen sc1\n\ts_waitcnt vmcnt(0)"
                         : "+v"(g0), "+v"(g1) : "v"(o0), "v"(o1), "s"(rd) : "memory");
            ok = (g0[1] == ta) & (g0[3] == tb) & (g1[1] == ta) & (g1[3] == tb);
        }
        if (__all(ok)) break;
        if constexpr (SLP > 0) __builtin_amdgcn_s_sleep(SLP);
    }
}
__device__ __forceinline__ float gemv16(const v4u (&w)[4], const float (&xr)[4], int lane) {
    float a[8] = {0, 0, 0, 0, 0, 0, 0, 0};
#pragma unroll
    for (int k = 0; k < 4; ++k)
#pragma unroll
        for (int i = 0; i < 4; ++i) { a[2 * i] = fmaf(xr[k], bf_lo(w[k][i]), a[2 * i]); a[2 * i + 1] = fmaf(xr[k], bf_hi(w[k][i]), a[2 * i + 1]); }
    const bool b1 = lane & 2;
    float t[4];
#pragma unroll
    for (int i = 0; i < 4; ++i) {
        t[i] = (b1 ? a[4 + i] : a[i]) + dppf<X2>(b1 ? a[i] : a[4 + i]);
        t[i] += dppf<ROR4>(t[i]); t[i] += dppf<ROR8>(t[i]);
    }
    return rs32(rs16(t[0], t[1]), rs16(t[2], t[3]));
}

struct __attribute__((aligned(16))) Lds {
    float sacc[NSTR][576];
    float xn[NAUX][D];
    float ss[NAUX][256];
    float redq[NAUX][16], redf[NAUX][16];
    unsigned q[256];
    unsigned h[64];
    float sm[NSTR][4], sl[NSTR][4];
    float wgt[32];
    unsigned tag[2];
    float gate[2];
    unsigned cq, cf, cml, fq, fwo, cs, fw, fg, fk;
};

__global__ void __launch_bounds__(NTHR, 2)
fused_step(const unsigned short* __restrict__ x, const unsigned short* __restrict__ kc, const unsigned short* __restrict__ vc,
           const unsigned short* __restrict__ wq, const unsigned short* __restrict__ wo, const unsigned short* __restrict__ wg,
           const unsigned short* __restrict__ wu, const unsigned short* __restrict__ wd, unsigned short* __restrict__ out, char* ws) {
    __shared__ Lds L;
    const int b = blockIdx.x, lane = threadIdx.x & 63, wv = __builtin_amdgcn_readfirstlane(threadIdx.x >> 6);
    const int xcd = b & 7, r = b >> 3;
    const int g = xcd, sp = r;
    const rsrc_t rs = __builtin_amdgcn_make_buffer_rsrc(ws, 0, WS_USED, 0x00020000);
    const v4u rd = {(unsigned)(uintptr_t)ws, (unsigned)((uintptr_t)ws >> 32) & 0xffffu, WS_USED, 0x00020000u};

    unsigned long long pc0; int cend0, cend1;
    asm volatile("s_getpc_b64 %0\n.Lfs_begin:\n\ts_mov_b32 %1, .Lfs_end0-.Lfs_begin\n\ts_mov_b32 %2, .Lfs_end1-.Lfs_begin" : "=s"(pc0), "=s"(cend0), "=s"(cend1));
    auto code_warm = [&]() __attribute__((always_inline)) {
        const int o = (r + 32 * lane) * 128;
        unsigned t = 0;
        if (o < max(cend0, cend1)) t = *(const GAS unsigned*)(pc0 + o);
        return t;
    };

    if (wv >= NAUX) {
        const int w = wv - NAUX;
        const unsigned base = (unsigned)(g * SK + sp * (TOKIL ? 16 : 1024) + w * 4) * (DH * 2);
        const rsrc_t rk = __builtin_amdgcn_make_buffer_rsrc((void*)kc, 0, 8u * SK * DH * 2, 0x00020000);
        const rsrc_t rv = __builtin_amdgcn_make_buffer_rsrc((void*)vc, 0, 8u * SK * DH * 2, 0x00020000);
        const unsigned ko = base + ((lane >> 2) & 3) * 256 + (lane >> 4) * 64 + (lane & 3) * 16, vo = base + lane * 16;
        const int qp = lane & 3;
        const bool o1 = qp & 1, o2 = qp & 2;
        const unsigned mk0 = qp == 0 ? 0xffffu : qp == 1 ? 0xffff0000u : 0u;
        const unsigned mk1 = qp == 2 ? 0xffffu : qp == 3 ? 0xffff0000u : 0u;
        v4u win[WIN]; v4u qb; float S[CH / 4]; unsigned pp[CH / 4];
        v4f acc0 = {0, 0, 0, 0}, acc1 = {0, 0, 0, 0};
        constexpr int VS = CH % WIN;
        static_assert(VS + DEP <= WIN && WIN + CH / 4 - 1 <= CH - 1 + DEP, "schedule");
        wg_barrier();
        sfor<0, CH + DEP>([&](auto mi) __attribute__((always_inline)) {
            constexpr int m = decltype(mi)::value;
            if constexpr (m >= WIN) wait_vm<DEP - 1>(); else if constexpr (m >= DEP0) wait_vm<DEP0 - 1>();
            if constexpr (m == QSIG) { if (w == 0) lds_signal(&L.fk); }
            if constexpr (m == WIN) {
                STAMP(3);
                lds_wait(&L.fq, 1);
                qb = *(const v4u*)&L.q[((lane >> 2) & 3) * 64 + 16 * (lane >> 4) + 4 * qp];
            }
            sfor<0, CH / 4>([&](auto gii) __attribute__((always_inline)) {
                constexpr int gi = decltype(gii)::value;
                if constexpr (m == cmax(4 * gi + 3 + DEP, WIN + gi)) {
                    float e[4];
#pragma unroll
                    for (int c = 0; c < 4; ++c) {
                        v4f d = __builtin_amdgcn_mfma_f32_16x16x32_bf16(__builtin_bit_cast(v8bf, win[(4 * gi + c) % WIN]), __builtin_bit_cast(v8bf, qb), (v4f){0, 0, 0, 0}, 0, 0, 0);
                        e[c] = o2 ? (o1 ? d[3] : d[2]) : (o1 ? d[1] : d[0]);
                    }
                    const float k01 = (o1 ? e[1] : e[0]) + dppf<X1>(o1 ? e[0] : e[1]);
                    const float k23 = (o1 ? e[3] : e[2]) + dppf<X1>(o1 ? e[2] : e[3]);
                    S[gi] = rbf(rbf((o2 ? k23 : k01) + dppf<X2>(o2 ? k01 : k23)) * SCALE);
                }
            });
            __builtin_amdgcn_sched_barrier(0);
            if constexpr (m < CH) win[m % WIN] = __builtin_amdgcn_raw_buffer_load_b128(rk, ko, m * CSTR, KV_AUX);
            else win[m % WIN] = __builtin_amdgcn_raw_buffer_load_b128(rv, vo, (m - CH) * CSTR, KV_AUX);
            __builtin_amdgcn_sched_barrier(0);
        });
        {
            float mx = S[0];
#pragma unroll
            for (int i = 1; i < CH / 4; ++i) mx = fmaxf(mx, S[i]);
            mx = fmaxf(mx, dppf<X1>(mx)); mx = fmaxf(mx, dppf<X2>(mx)); mx = mx32(mx16(mx));
            const float mc = mx * LOG2E;
            float l = 0;
#pragma unroll
            for (int i = 0; i < CH / 4; ++i) { const float p = __builtin_amdgcn_exp2f(fmaf(S[i], LOG2E, -mc)); l += p; pp[i] = cvt_pk_bf16(p, p); }
            l += dppf<X1>(l); l += dppf<X2>(l); l = rs16(l, l); l = rs32(l, l);
            if (lane < 16 && qp == 0) { L.sm[w][lane >> 2] = mc; L.sl[w][lane >> 2] = l; }
            lds_signal(&L.cml);
            STAMP(4);
        }
        auto pv = [&](auto ji) __attribute__((always_inline)) {
            constexpr int j = decltype(ji)::value;
            const unsigned pb = dppu<(j & 3) * 0x55>(pp[j >> 2]);
            const v2u a = {pb & mk0, pb & mk1};
            const v4u v = win[VS + j];
            const v2u vlo = {v[0], v[1]}, vhi = {v[2], v[3]};
            acc0 = __builtin_amdgcn_mfma_f32_16x16x16bf16_1k(__builtin_bit_cast(v4s, a), __builtin_bit_cast(v4s, vlo), acc0, 0, 0, 0);
            acc1 = __builtin_amdgcn_mfma_f32_16x16x16bf16_1k(__builtin_bit_cast(v4s, a), __builtin_bit_cast(v4s, vhi), acc1, 0, 0, 0);
        };
#pragma nounroll
        for (int it = 1; it < CH / DEP; ++it) {
            sfor<0, DEP>([&](auto ji) __attribute__((always_inline)) {
                constexpr int j = decltype(ji)::value;
                pv(ji);
                __builtin_amdgcn_sched_barrier(0);
                win[VS + j] = __builtin_amdgcn_raw_buffer_load_b128(rv, vo, (it * DEP + j) * CSTR, KV_AUX);
                __builtin_amdgcn_sched_barrier(0);
            });
#pragma unroll
            for (int i = 0; i + DEP / 4 < CH / 4; ++i) pp[i] = pp[i + DEP / 4];
            if (it == WO_IT) lds_signal(&L.fwo);
        }
        lds_wait(&L.cml, NSTR);
        const int h = lane >> 4;
        const float f = __builtin_amdgcn_exp2f(L.sm[w][h] - fmaxf(fmaxf(L.sm[0][h], L.sm[1][h]), fmaxf(L.sm[2][h], L.sm[3][h])));
        sfor<0, DEP>(pv);
        *(v4f*)&L.sacc[w][4 * lane] = acc0 * f;
        *(v4f*)&L.sacc[w][288 + 4 * lane] = acc1 * f;
        STAMP(6);
        lds_signal(&L.cs);
        lds_wait(&L.cs, NSTR);
        {
            const int t = w * 64 + lane;
            const int idx = ((t >> 1) & 1) * 288 + (t >> 2) * 4 + (t & 1) * 2;
            v2f s = *(const v2f*)&L.sacc[0][idx];
#pragma unroll
            for (int i = 1; i < NSTR; ++i) s += *(const v2f*)&L.sacc[i][idx];
            put(rs, OFF_O + (g * 32 + sp) * 4096 + t * 16, __float_as_uint(s[0]), __float_as_uint(s[1]), L.tag[0], L.tag[1]);
            STAMP(7);
        }
        if (w == 0) FLUSH(0xd8);
        asm volatile(".Lfs_end0:");
        return;
    }

    const int a = wv;
    STAMP(0);
    unsigned cw = 0;
    if (a == 1) cw = code_warm();
    const v2u ev = __builtin_amdgcn_raw_buffer_load_b64(rs, OFF_E, 0, 16);
    const v4u xv = *(const GAS v4u*)((gptr)x + lane * 16);
    const int qs = xcd * 32 + r;
    v4u wqv[4], wfv[4];
    {
        gptr p = (gptr)wq + (size_t)(128 * a + (lane >> 1)) * (QN * 2) + qs * 32 + (lane & 1) * 16;
#pragma unroll
        for (int k = 0; k < 4; ++k) wqv[k] = ldw(p + k * (32 * QN * 2));
    }
    if (threadIdx.x == 0) { L.cq = 0; L.cf = 0; L.cml = 0; L.fq = 0; L.fwo = 0; L.cs = 0; L.fw = 0; L.fg = 0; L.fk = 0; }
    __builtin_amdgcn_sched_barrier(0);
    wg_barrier();
    const bool ffn = r < 28;
    const int fs = xcd * 28 + (ffn ? r : 27);
    const unsigned ta = __builtin_amdgcn_readfirstlane(ev[0]) + 1u, tb = __builtin_amdgcn_readfirstlane(ev[1]) + 1u;
    float xr[4];
    {
        float xf[8];
#pragma unroll
        for (int i = 0; i < 4; ++i) { xf[2 * i] = bf_lo(xv[i]); xf[2 * i + 1] = bf_hi(xv[i]); }
        float s = 0;
#pragma unroll
        for (int i = 0; i < 8; ++i) s += xf[i];
        const float mean = wave_sum(s) * (1.f / D);
        float q2 = 0;
#pragma unroll
        for (int i = 0; i < 8; ++i) { const float d = xf[i] - mean; q2 = fmaf(d, d, q2); }
        const float var = wave_sum(q2) * (1.f / D);
        const float mb = rbf(mean), sd = rbf(sqrtf(rbf(rbf(var) + 1e-5f)));
        v4f n0, n1;
#pragma unroll
        for (int i = 0; i < 4; ++i) { n0[i] = rbf(rbf(xf[i] - mb) / sd); n1[i] = rbf(rbf(xf[4 + i] - mb) / sd); }
        *(v4f*)&L.xn[a][8 * lane] = n0; *(v4f*)&L.xn[a][8 * lane + 4] = n1;
        cbar();
#pragma unroll
        for (int k = 0; k < 4; ++k) xr[k] = L.xn[a][128 * a + 32 * k + (lane >> 1)];
    }
    const int rc = 8 * (lane & 1) + 4 * ((lane >> 1) & 1) + 2 * (lane >> 5) + ((lane >> 4) & 1);
    {
        const float v = gemv16(wqv, xr, lane);
        if ((lane & 12) == 0) L.redq[a][rc] = v;
        lds_signal(&L.cq);
        asm volatile("" :: "v"(cw));
    }
    if (a == 0) {
        lds_wait(&L.cq, NAUX);
        if (lane < 4) {
            const v4f s = *(const v4f*)&L.redq[0][4 * lane] + *(const v4f*)&L.redq[1][4 * lane] + *(const v4f*)&L.redq[2][4 * lane] + *(const v4f*)&L.redq[3][4 * lane];
            put(rs, OFF_Q + (qs * 4 + lane) * 16, cvt_pk_bf16(s[0], s[1]), cvt_pk_bf16(s[2], s[3]), ta, tb);
        }
        STAMP(1);
    }
    __builtin_amdgcn_sched_barrier(0);
    {
        gptr p = (gptr)((lane & 1) ? wu : wg) + (size_t)(128 * a + (lane >> 1)) * (DF * 2) + fs * 16;
#pragma unroll
        for (int k = 0; k < 4; ++k) wfv[k] = ldw(p + k * (32 * DF * 2));
    }
    __builtin_amdgcn_sched_barrier(0);
    {
        const float v = gemv16(wfv, xr, lane);
        if ((lane & 12) == 0) L.redf[a][rc] = v;
        lds_signal(&L.cf);
    }
    const int rb = sp >> 1, chh = sp & 1;
    unsigned x2 = 0;
    if (a == 0) {
        lds_wait(&L.fk, 1);
        v4u gq[2]; const unsigned off[2] = {OFF_Q + (g * 128 + lane) * 16, OFF_Q + (g * 128 + 64 + lane) * 16};
        gather2<SLP_Q>(rd, off[0], off[1], gq[0], gq[1], ta, tb);
        *(v2u*)&L.q[2 * lane] = (v2u){gq[0][0], gq[0][2]};
        *(v2u*)&L.q[128 + 2 * lane] = (v2u){gq[1][0], gq[1][2]};
        if (lane == 0) { L.tag[0] = ta; L.tag[1] = tb; }
        lds_signal(&L.fq);
        STAMP(2);
    } else if (a == 1) {
        lds_wait(&L.cf, NAUX);
        if (lane < 2 && ffn) {
            const v4f gs = *(const v4f*)&L.redf[0][4 * lane] + *(const v4f*)&L.redf[1][4 * lane] + *(const v4f*)&L.redf[2][4 * lane] + *(const v4f*)&L.redf[3][4 * lane];
            const v4f us = *(const v4f*)&L.redf[0][8 + 4 * lane] + *(const v4f*)&L.redf[1][8 + 4 * lane] + *(const v4f*)&L.redf[2][8 + 4 * lane] + *(const v4f*)&L.redf[3][8 + 4 * lane];
            float hv[4];
#pragma unroll
            for (int i = 0; i < 4; ++i) {
                const float gg = rbf(gs[i]), uu = rbf(us[i]);
                const float th = 1.f - 2.f / (__expf(1.5957691216057308f * (gg + 0.044715f * gg * gg * gg)) + 1.f);
                hv[i] = rbf(0.5f * gg * (1.f + th)) * uu;
            }
            put(rs, OFF_H + (fs * 2 + lane) * 16, cvt_pk_bf16(hv[0], hv[1]), cvt_pk_bf16(hv[2], hv[3]), ta, tb);
        }
    } else if (a == 3) {
        const int drb = qs >> 4, dcb = qs & 15;
        v4u wdv[7];
        gptr p = (gptr)wd + (size_t)(112 * drb + (lane >> 2)) * (D * 2) + dcb * 64 + (lane & 3) * 16;
#pragma unroll
        for (int k = 0; k < 7; ++k) wdv[k] = ldw(p + k * (16 * D * 2));
        x2 = *(const GAS unsigned*)((gptr)x + b * 4);
        v4u gh[1]; const unsigned off[1] = {OFF_H + (28 * drb + (lane < 28 ? lane : 27)) * 16};
        lds_wait(&L.cml, NSTR);
        gather1<SLP_F>(rd, off[0], gh[0], ta, tb);
        if (lane < 28) *(v2u*)&L.h[2 * lane] = (v2u){gh[0][0], gh[0][2]};
        cbar();
        float c[8] = {0, 0, 0, 0, 0, 0, 0, 0};
#pragma unroll
        for (int k = 0; k < 7; ++k) {
            const float hv = bf_lo(((const unsigned short*)L.h)[16 * k + (lane >> 2)]);
#pragma unroll
            for (int i = 0; i < 4; ++i) { c[2 * i] = fmaf(hv, bf_lo(wdv[k][i]), c[2 * i]); c[2 * i + 1] = fmaf(hv, bf_hi(wdv[k][i]), c[2 * i + 1]); }
        }
#pragma unroll
        for (int i = 0; i < 8; ++i) { c[i] += dppf<ROR4>(c[i]); c[i] += dppf<ROR8>(c[i]); }
        const float w0 = rs32(rs16(c[0], c[2]), rs16(c[4], c[6])), w1 = rs32(rs16(c[1], c[3]), rs16(c[5], c[7]));
        if ((lane & 12) == 0) put(rs, OFF_D + (qs * 16 + 4 * (lane & 3) + 2 * (lane >> 5) + ((lane >> 4) & 1)) * 16, __float_as_uint(w0), __float_as_uint(w1), ta, tb);
        STAMP(12);
    }
    if (a == 2) {
        lds_wait(&L.cml, NSTR);
        const float m = L.sm[lane & 3][(lane >> 2) & 3], l = L.sl[lane & 3][(lane >> 2) & 3];
        float M = fmaxf(m, dppf<X1>(m)); M = fmaxf(M, dppf<X2>(M));
        float s = l * __builtin_amdgcn_exp2f(m - M);
        s += dppf<X1>(s); s += dppf<X2>(s);
        if (lane < 16 && (lane & 3) == 0) put(rs, OFF_ML + ((g * 4 + (lane >> 2)) * 32 + sp) * 16, __float_as_uint(M), __float_as_uint(s), ta, tb);
    }
    lds_wait(&L.fwo, 1);
    if (a == 1) cw = code_warm();
    float wof[4][8];
    {
        gptr p = (gptr)wo + (size_t)(512 * g + 32 * rb + 8 * a + (lane >> 5)) * (D * 2) + chh * 512 + (lane & 31) * 16;
        v4u t[4];
#pragma unroll
        for (int k = 0; k < 4; ++k) t[k] = ldw(p + k * (2 * D * 2));
#pragma unroll
        for (int k = 0; k < 4; ++k)
#pragma unroll
            for (int i = 0; i < 4; ++i) { wof[k][2 * i] = bf_lo(t[k][i]); wof[k][2 * i + 1] = bf_hi(t[k][i]); }
    }
#pragma unroll
    for (int k = 0; k < 4; ++k)
#pragma unroll
        for (int i = 0; i < 8; ++i) asm volatile("" : "+v"(wof[k][i]));
    asm volatile("" :: "v"(cw));
    if (a == 2) {
        v4u gm;
        gather1<SLP_F>(rd, OFF_ML + ((g * 4 + (rb >> 2)) * 32 + (lane & 31)) * 16, gm, ta, tb);
        const float m0 = __uint_as_float(gm[0]);
        float G = fmaxf(m0, dppf<X1>(m0)); G = fmaxf(G, dppf<X2>(G)); G = fmaxf(G, dppf<ROR4>(G)); G = fmaxf(G, dppf<ROR8>(G)); G = mx16(G);
        const float e = __builtin_amdgcn_exp2f(m0 - G);
        float T = __uint_as_float(gm[2]) * e;
        T += dppf<X1>(T); T += dppf<X2>(T); T += dppf<ROR4>(T); T += dppf<ROR8>(T); T = rs16(T, T);
        if (lane < 32) L.wgt[lane] = e / T;
        lds_signal(&L.fw);
        STAMP(5);
    }
    lds_wait(&L.fw, 1);
    lds_wait(&L.cs, NSTR);
    {
        const unsigned o0 = OFF_O + (g * 32 + (lane >> 2)) * 4096 + (16 * rb + 4 * a + (lane & 3)) * 16;
        v4u go[2]; const unsigned off[2] = {o0, o0 + 16 * 4096};
        const float wa = L.wgt[lane >> 2], wb = L.wgt[16 + (lane >> 2)];
        gather2<SLP_H>(rd, off[0], off[1], go[0], go[1], ta, tb);
        STAMP(8);
        float v0 = fmaf(__uint_as_float(go[0][0]), wa, __uint_as_float(go[1][0]) * wb), v1 = fmaf(__uint_as_float(go[0][2]), wa, __uint_as_float(go[1][2]) * wb);
        v0 += dppf<ROR4>(v0); v1 += dppf<ROR4>(v1); v0 += dppf<ROR8>(v0); v1 += dppf<ROR8>(v1);
        const float o = rbf(rs32(rs16(v0, v0), rs16(v1, v1)));
        const float ok[4] = {dppf<0x00>(o), dppf<0x55>(o), dppf<0xAA>(o), dppf<0xFF>(o)};
        float c[8];
#pragma unroll
        for (int i = 0; i < 8; ++i) c[i] = ok[0] * wof[0][i] + ok[1] * wof[1][i] + ok[2] * wof[2][i] + ok[3] * wof[3][i];
        v4f s4;
#pragma unroll
        for (int i = 0; i < 4; ++i) s4[i] = rs32(c[i], c[4 + i]);
        *(v4f*)&L.ss[a][8 * (lane & 31) + 4 * (lane >> 5)] = s4;
    }
    wg_barrier();
    if (a < 2) {
        const int n = 64 * a + lane;
        const v2f s = *(const v2f*)&L.ss[0][2 * n] + *(const v2f*)&L.ss[1][2 * n] + *(const v2f*)&L.ss[2][2 * n] + *(const v2f*)&L.ss[3][2 * n];
        put(rs, OFF_S + (chh * 128 + g * 16 + rb) * 2048 + n * 16, __float_as_uint(s[0]), __float_as_uint(s[1]), ta, tb);
        STAMP(9);
    } else if (a == 2) {
        v4u gd;
        gather1<SLP_H>(rd, OFF_D + (((lane & 15) * 16 + (b >> 4)) * 16 + (b & 15)) * 16, gd, ta, tb);
        float d0 = __uint_as_float(gd[0]), d1 = __uint_as_float(gd[2]);
        d0 += dppf<X1>(d0); d0 += dppf<X2>(d0); d0 += dppf<ROR4>(d0); d0 += dppf<ROR8>(d0);
        d1 += dppf<X1>(d1); d1 += dppf<X2>(d1); d1 += dppf<ROR4>(d1); d1 += dppf<ROR8>(d1);
        const float dd = rbf(lane >= 32 ? d1 : d0);
        if ((lane & 31) == 0) L.gate[lane >> 5] = rbf(dd / (1.f + __expf(-dd)));
        lds_signal(&L.fg);
    } else {
        const unsigned o0 = OFF_S + ((b >> 7) * 128 + lane) * 2048 + (b & 127) * 16;
        v4u gs[2]; const unsigned off[2] = {o0, o0 + 64 * 2048};
        gather2<SLP_H>(rd, off[0], off[1], gs[0], gs[1], ta, tb);
        STAMP(10);
        float v0 = __uint_as_float(gs[0][0]) + __uint_as_float(gs[1][0]), v1 = __uint_as_float(gs[0][2]) + __uint_as_float(gs[1][2]);
        v0 += dppf<X1>(v0); v1 += dppf<X1>(v1); v0 += dppf<X2>(v0); v1 += dppf<X2>(v1);
        v0 += dppf<ROR4>(v0); v1 += dppf<ROR4>(v1); v0 += dppf<ROR8>(v0); v1 += dppf<ROR8>(v1);
        const float s = rbf(rs32(rs16(v0, v0), rs16(v1, v1)));
        lds_wait(&L.fg, 1);
        const float res = (lane >= 32 ? bf_hi(x2) : bf_lo(x2)) + rbf(s * L.gate[lane >> 5]);
        if ((lane & 31) == 0) out[2 * b + (lane >> 5)] = (unsigned short)(cvt_pk_bf16(res, res) & 0xffffu);
        if (b == 0 && lane == 0) __builtin_amdgcn_raw_buffer_store_b64((v2u){ta, tb}, rs, OFF_E, 0, 16);
        STAMP(11);
    }
    if (a == 0) FLUSH(0x307);
    if (a == 2) FLUSH(0x20);
    if (a == 3) FLUSH(0x1c00);
    asm volatile(".Lfs_end1:");
}

extern "C" void kernel_launch(void* const* d_in, const int* in_sizes, int n_in, void* d_out, int out_size, void* d_ws, size_t ws_size, hipStream_t stream) {
    fused_step<<<256, NTHR, 0, stream>>>((const unsigned short*)d_in[0], (const unsigned short*)d_in[1], (const unsigned short*)d_in[2],
                                         (const unsigned short*)d_in[3], (const unsigned short*)d_in[4], (const unsigned short*)d_in[6],
                                         (const unsigned short*)d_in[7], (const unsigned short*)d_in[8], (unsigned short*)d_out, (char*)d_ws);
}
```

```cpp
#include <hip/hip_runtime.h>
#include <stdint.h>

#define GAS __attribute__((address_space(1)))
typedef unsigned v4u __attribute__((ext_vector_type(4)));
typedef unsigned v16u __attribute__((ext_vector_type(16)));
typedef unsigned v2u __attribute__((ext_vector_type(2)));
typedef float v4f __attribute__((ext_vector_type(4)));
typedef float v2f __attribute__((ext_vector_type(2)));
typedef short v4s __attribute__((ext_vector_type(4)));
typedef __bf16 v8bf __attribute__((ext_vector_type(8)));
typedef const GAS char* gptr;

constexpr int D = 512, SK = 32768, DH = 128, DF = 1792, QN = 4096;
constexpr int NAUX = 4, NSTR = 4, NTHR = 64 * (NAUX + NSTR);
constexpr int CH = 64;
#define WIN 56
#define DEP0 10
#define TOKIL 0
constexpr int CSTR = TOKIL ? 32 * NSTR * 1024 : NSTR * 1024;
#define DEP 16
#define DEPV DEP
#define QSIG 40
#define SLP_Q 16
#define SLP_F 32
#define SLP_H 2
#define KV_AUX 2
#define W_NT 1
#define BARPOS 0
#define FASTT 64
#define STAG 6
#define STAG2 STAG
#define ML_IT 1
#define WO_N 4
#define WO_IT 4
constexpr float SCALE = 0.08838834764831845f, LOG2E = 1.4426950408889634f;

constexpr unsigned OFF_E = 0;
constexpr unsigned OFF_ML = 4096;
constexpr unsigned OFF_Q = OFF_ML + 16384;
constexpr unsigned OFF_H = OFF_Q + 131072;
constexpr unsigned OFF_D = OFF_H + 131072;
constexpr unsigned OFF_O = OFF_D + 131072;
constexpr unsigned OFF_S = OFF_O + (1u << 20);
constexpr unsigned WS_USED = OFF_S + (512u << 10);
constexpr unsigned SPIN_CAP = 1u << 15;
#define ECHO_SEL 1
#define STAMP(k) do { } while (0)
#define FLUSH(mask) do { } while (0)

__device__ __forceinline__ float bf_lo(unsigned v) { return __uint_as_float(v << 16); }
__device__ __forceinline__ float bf_hi(unsigned v) { return __uint_as_float(v & 0xffff0000u); }
__device__ __forceinline__ unsigned cvt_pk_bf16(float lo, float hi) {
    unsigned r; asm("v_cvt_pk_bf16_f32 %0, %1, %2" : "=v"(r) : "v"(lo), "v"(hi)); return r;
}
__device__ __forceinline__ float rbf(float x) { return bf_hi(cvt_pk_bf16(x, x)); }
template <int CTRL> __device__ __forceinline__ float dppf(float v) {
    return __int_as_float(__builtin_amdgcn_update_dpp(0, __float_as_int(v), CTRL, 0xf, 0xf, true));
}
template <int CTRL> __device__ __forceinline__ unsigned dppu(unsigned v) {
    return (unsigned)__builtin_amdgcn_update_dpp(0, (int)v, CTRL, 0xf, 0xf, true);
}
constexpr int X1 = 0xB1, X2 = 0x4E, ROR4 = 0x124, ROR8 = 0x128, HMIR = 0x141;
__device__ __forceinline__ float rs16(float a, float b) {
    auto r = __builtin_amdgcn_permlane16_swap(__float_as_uint(a), __float_as_uint(b), false, false);
    return __uint_as_float(r[0]) + __uint_as_float(r[1]);
}
__device__ __forceinline__ float rs32(float a, float b) {
    auto r = __builtin_amdgcn_permlane32_swap(__float_as_uint(a), __float_as_uint(b), false, false);
    return __uint_as_float(r[0]) + __uint_as_float(r[1]);
}
__device__ __forceinline__ float mx16(float a) {
    auto r = __builtin_amdgcn_permlane16_swap(__float_as_uint(a), __float_as_uint(a), false, false);
    return fmaxf(__uint_as_float(r[0]), __uint_as_float(r[1]));
}
__device__ __forceinline__ float mx32(float a) {
    auto r = __builtin_amdgcn_permlane32_swap(__float_as_uint(a), __float_as_uint(a), false, false);
    return fmaxf(__uint_as_float(r[0]), __uint_as_float(r[1]));
}
__device__ __forceinline__ float wave_sum(float v) {
    v += dppf<X1>(v); v += dppf<X2>(v); v += dppf<ROR4>(v); v += dppf<ROR8>(v);
    v = rs16(v, v); return rs32(v, v);
}
template <int N> __device__ __forceinline__ void wait_vm() { asm volatile("s_waitcnt vmcnt(%0)" :: "n"(N) : "memory"); }
__device__ __forceinline__ void cbar() { asm volatile("" ::: "memory"); }
__device__ __forceinline__ void wg_barrier() { asm volatile("s_waitcnt lgkmcnt(0)\n\ts_barrier" ::: "memory"); }
__device__ __forceinline__ v4u ldw(gptr p) {
    return W_NT ? __builtin_nontemporal_load((const GAS v4u*)p) : *(const GAS v4u*)p; }
__device__ __forceinline__ void lds_signal(unsigned* w) {
    asm volatile("s_waitcnt lgkmcnt(0)" ::: "memory");
    if ((threadIdx.x & 63) == 0) __hip_atomic_fetch_add(w, 1u, __ATOMIC_RELAXED, __HIP_MEMORY_SCOPE_WORKGROUP);
    cbar();
}
__device__ __forceinline__ void lds_wait(unsigned* w, unsigned n) {
    cbar();
    while ((unsigned)__builtin_amdgcn_readfirstlane(__hip_atomic_load(w, __ATOMIC_RELAXED, __HIP_MEMORY_SCOPE_WORKGROUP)) < n) __builtin_amdgcn_s_sleep(1);
    cbar();
}
template <int V> struct IC { static constexpr int value = V; };
template <int I, int N, class F> __device__ __forceinline__ void sfor(F&& f) { if constexpr (I < N) { f(IC<I>{}); sfor<I + 1, N>(f); } }
constexpr int cmax(int a, int b) { return a > b ? a : b; }
typedef __amdgpu_buffer_rsrc_t rsrc_t;
__device__ __forceinline__ void put(rsrc_t rs, unsigned off, unsigned v0, unsigned v1, unsigned ta, unsigned tb) {
    __builtin_amdgcn_raw_buffer_store_b128((v4u){v0, ta, v1, tb}, rs, off, 0, 16);
}
template <int SLP> __device__ __forceinline__ void gather1(v4u rd, unsigned o0, v4u& g0, unsigned ta, unsigned tb) {
    bool ok = false;
    g0 = (v4u){0, 0, 0, 0};
#pragma nounroll
    for (unsigned spins = 0; spins < SPIN_CAP; ++spins) {
        if (!ok) {
            asm volatile("buffer_load_dwordx4 %0, %1, %2, 0 offen sc1\n\ts_waitcnt vmcnt(0)" : "+v"(g0) : "v"(o0), "s"(rd) : "memory");
            ok = (g0[1] == ta) & (g0[3] == tb);
        }
        if (__all(ok)) break;
        if constexpr (SLP > 0) __builtin_amdgcn_s_sleep(SLP);
    }
}
struct G4 { v4u g[4]; };
template <int N> __device__ __forceinline__ void gissue(G4& s, v4u rd, unsigned o, unsigned st) {
    if constexpr (N == 2)
        asm volatile("buffer_load_dwordx4 %0, %2, %3, 0 offen sc1\n\tbuffer_load_dwordx4 %1, %2, %3, %4 offen sc1"
                     : "+v"(s.g[0]), "+v"(s.g[1]) : "v"(o), "s"(rd), "s"(st) : "memory");
    else
        asm volatile("buffer_load_dwordx4 %0, %4, %5, 0 offen sc1\n\tbuffer_load_dwordx4 %1, %4, %5, %6 offen sc1\n\t"
                     "buffer_load_dwordx4 %2, %4, %5, %7 offen sc1\n\tbuffer_load_dwordx4 %3, %4, %5, %8 offen sc1"
                     : "+v"(s.g[0]), "+v"(s.g[1]), "+v"(s.g[2]), "+v"(s.g[3]) : "v"(o), "s"(rd), "s"(st), "s"(2 * st), "s"(3 * st) : "memory");
}
template <int W> __device__ __forceinline__ void gwait(G4& s) {
    asm volatile("s_waitcnt vmcnt(%4)" : "+v"(s.g[0]), "+v"(s.g[1]), "+v"(s.g[2]), "+v"(s.g[3]) : "n"(W) : "memory");
}
template <int N> __device__ __forceinline__ bool gvalid(const G4& s, unsigned ta, unsigned tb) {
    bool v = true;
#pragma unroll
    for (int i = 0; i < N; ++i) v = v & (s.g[i][1] == ta) & (s.g[i][3] == tb);
    return v;
}
template <int SLP> __device__ __forceinline__ void gather4(v4u rd, unsigned o, unsigned st, G4& s, unsigned ta, unsigned tb) {
    bool ok = false;
#pragma nounroll
    for (unsigned spins = 0; spins < SPIN_CAP; ++spins) {
        if (!ok) { gissue<4>(s, rd, o, st); gwait<0>(s); ok = gvalid<4>(s, ta, tb); }
        if (__all(ok)) break;
        __builtin_amdgcn_s_sleep(SLP);
    }
}
#define PA 200
#define PB 216
#define PC 232
#define S_(x) #x
#define VR(B, i) "v[" S_(B) "+" #i "]"
#define VQ(B, i) "v[" S_(B) "+" #i ":" S_(B) "+" #i "+3]"
#define CK(B, i, j) "v_cmpx_eq_u32_e32 vcc, %[ta], " VR(B, i) "\n\tv_cmpx_eq_u32_e32 vcc, %[tb], " VR(B, j) "\n\t"
#define RETIRE "s_andn2_b64 %[ms], %[ms], exec\n\ts_cbranch_scc0 .Lgp_done%=\n\ts_mov_b64 exec, %[ms]\n\t"
#define LD2(B) "buffer_load_dwordx4 " VQ(B, 0) ", %[o], %[rd], 0 offen sc1\n\tbuffer_load_dwordx4 " VQ(B, 4) ", %[o], %[rd], %[st] offen sc1\n\t"
#define CK2(B) CK(B, 1, 3) CK(B, 5, 7) \
    "v_mul_f32 %[v0], " VR(B, 4) ", %[wb]\n\tv_mul_f32 %[v1], " VR(B, 6) ", %[wb]\n\t" \
    "v_fmac_f32 %[v0], " VR(B, 0) ", %[wa]\n\tv_fmac_f32 %[v1], " VR(B, 2) ", %[wa]\n\t" RETIRE
#define ER(B, i) "v[(" S_(B) "-200)/4+184+" #i "]"
#define LD5(B) "buffer_load_dwordx4 v[(" S_(B) "-200)/4+184:(" S_(B) "-200)/4+187], %[od], %[rd], 0 offen sc1\n\tbuffer_load_dwordx4 " VQ(B, 0) ", %[o], %[rd], 0 offen sc1\n\tbuffer_load_dwordx4 " VQ(B, 4) ", %[o], %[rd], 0 offen offset:1024 sc1\n\t" \
    "buffer_load_dwordx4 " VQ(B, 8) ", %[o], %[rd], 0 offen offset:2048 sc1\n\tbuffer_load_dwordx4 " VQ(B, 12) ", %[o], %[rd], 0 offen offset:3072 sc1\n\t"
#define CK5(B) CK(B, 1, 3) CK(B, 5, 7) CK(B, 9, 11) CK(B, 13, 15) \
    "v_cmpx_eq_u32_e32 vcc, %[ta], " ER(B, 1) "\n\tv_cmpx_eq_u32_e32 vcc, %[tb], " ER(B, 3) "\n\t" \
    "v_mov_b32 %[d0], " ER(B, 0) "\n\tv_mov_b32 %[d1], " ER(B, 2) "\n\t" \
    "v_add_f32 %[v0], " VR(B, 0) ", " VR(B, 4) "\n\tv_add_f32 %[v1], " VR(B, 2) ", " VR(B, 6) "\n\t" \
    "v_add_f32 " VR(B, 8) ", " VR(B, 8) ", " VR(B, 12) "\n\tv_add_f32 " VR(B, 10) ", " VR(B, 10) ", " VR(B, 14) "\n\t" \
    "v_add_f32 %[v0], %[v0], " VR(B, 8) "\n\tv_add_f32 %[v1], %[v1], " VR(B, 10) "\n\t" RETIRE
#define PIPE(LD, CKN, W) ".Lgp_fast%=:\n\t" LD(PA) "s_sleep %[sg]\n\t" LD(PB) "s_sleep %[sg]\n\t" LD(PC) \
    ".Lgp_loop%=:\n\t" \
    "s_waitcnt vmcnt(" #W ")\n\t" CKN(PA) LD(PA) "s_waitcnt vmcnt(" #W ")\n\t" CKN(PB) LD(PB) "s_waitcnt vmcnt(" #W ")\n\t" CKN(PC) LD(PC) \
    "s_sub_u32 %[n], %[n], 1\n\ts_cbranch_scc0 .Lgp_loop%=\n\t" \
    ".Lgp_done%=:\n\ts_mov_b64 exec, %[sv]"
__device__ __forceinline__ void gatherp2(v4u rd, unsigned o, unsigned st, v16u& A, v16u& B, v16u& C, v16u& W, float wa, float wb, float& v0, float& v1, unsigned ta, unsigned tb) {
    unsigned long long sv, ms; unsigned n, t;
    asm volatile("s_mov_b64 %[sv], exec\n\ts_mov_b64 %[ms], exec\n\ts_mov_b32 %[n], %[cap]\n\ts_nop 1\n\t"
                 ".Lgp_slow%=:\n\t" LD2(PA) "s_waitcnt vmcnt(0)\n\t" CK2(PA)
                 "s_bcnt1_i32_b64 %[t], %[ms]\n\ts_cmp_le_u32 %[t], %[ft]\n\ts_cbranch_scc1 .Lgp_fast%=\n\t"
                 "s_sleep %[sl]\n\ts_sub_u32 %[n], %[n], 1\n\ts_cbranch_scc0 .Lgp_slow%=\n\ts_branch .Lgp_done%=\n\t"
                 PIPE(LD2, CK2, 4)
                 : "={v[200:215]}"(A), "={v[216:231]}"(B), "={v[232:247]}"(C), "+{v[184:199]}"(W), [v0] "=&v"(v0), [v1] "=&v"(v1), [sv] "=&s"(sv), [ms] "=&s"(ms), [n] "=&s"(n), [t] "=&s"(t)
                 : [o] "v"(o), [rd] "s"(rd), [st] "s"(st), [ta] "s"(ta), [tb] "s"(tb), [wa] "v"(wa), [wb] "v"(wb), [cap] "n"(SPIN_CAP), [ft] "n"(FASTT), [sl] "n"(SLP_H), [sg] "n"(STAG)
                 : "vcc", "scc", "memory");
}
__device__ __forceinline__ void gatherp5(v4u rd, unsigned o, unsigned od, v16u& A, v16u& B, v16u& C, v16u& W, float& v0, float& v1, float& d0, float& d1, unsigned ta, unsigned tb) {
    unsigned long long sv, ms; unsigned n;
    asm volatile("s_mov_b64 %[sv], exec\n\ts_mov_b64 %[ms], exec\n\ts_mov_b32 %[n], %[cap]\n\ts_nop 1\n\t"
                 PIPE(LD5, CK5, 10)
                 : "+{v[200:215]}"(A), "+{v[216:231]}"(B), "+{v[232:247]}"(C), "+{v[184:199]}"(W), [v0] "=&v"(v0), [v1] "=&v"(v1), [d0] "=&v"(d0), [d1] "=&v"(d1), [sv] "=&s"(sv), [ms] "=&s"(ms), [n] "=&s"(n)
                 : [o] "v"(o), [od] "v"(od), [rd] "s"(rd), [ta] "s"(ta), [tb] "s"(tb), [cap] "n"(SPIN_CAP), [sg] "n"(STAG2)
                 : "vcc", "scc", "memory");
}
template <int K> __device__ __forceinline__ void gemv64(const v4u (&w)[K], const float (&xr)[K], int lane, float& u0, float& u1) {
    float a[8] = {0, 0, 0, 0, 0, 0, 0, 0};
#pragma unroll
    for (int k = 0; k < K; ++k)
#pragma unroll
        for (int i = 0; i < 4; ++i) { a[2 * i] = fmaf(xr[k], bf_lo(w[k][i]), a[2 * i]); a[2 * i + 1] = fmaf(xr[k], bf_hi(w[k][i]), a[2 * i + 1]); }
    const bool b3 = lane & 8;
    float t[4];
#pragma unroll
    for (int i = 0; i < 4; ++i) t[i] = (b3 ? a[4 + i] : a[i]) + dppf<ROR8>(b3 ? a[i] : a[4 + i]);
    u0 = rs16(t[0], t[2]); u1 = rs16(t[1], t[3]);
}

struct __attribute__((aligned(16))) Lds {
    float sacc[NSTR][576];
    float xn[NAUX][D];
    float ss[NAUX][256];
    float redq[NAUX][64], redf[NAUX][128];
    unsigned q[256];
    float hs[2][64], hv[64];
    float sm[NSTR][4], sl[NSTR][4];
    float wgt[32];
    float ds[NAUX][16], fin[NAUX][16];
    unsigned tag[2];
    unsigned cq, cf, cml, fq, fwo, cs, fw, fk, st1, fm, fh;
};

__global__ void __launch_bounds__(NTHR, 2)
fused_step(const void*, const void*, const void*, const void*, const void*, const void*, const void*, const void*, void*, void*) {
    __shared__ Lds L;
    v16u ka; v4u kb;
    asm volatile("s_load_dwordx16 %0, %2, 0x0\n\ts_load_dwordx4 %1, %2, 0x40\n\ts_waitcnt lgkmcnt(0)" : "=&s"(ka), "=&s"(kb) : "s"(__builtin_amdgcn_kernarg_segment_ptr()));
    auto kp = [&](int i) __attribute__((always_inline)) { return (GAS char*)(((unsigned long long)ka[2 * i + 1] << 32) | ka[2 * i]); };
    const gptr x = kp(0), wq = kp(3), wo = kp(4), wg = kp(5), wu = kp(6), wd = kp(7);
    GAS char* const out = (GAS char*)(((unsigned long long)kb[1] << 32) | kb[0]);
    GAS char* const ws = (GAS char*)(((unsigned long long)kb[3] << 32) | kb[2]);
    const int b = blockIdx.x, lane = threadIdx.x & 63, wv = __builtin_amdgcn_readfirstlane(threadIdx.x >> 6);
#define GSW 0
    const int xcd = (b & 7) ^ GSW, r = b >> 3;
    const int g = xcd, sp = r;
    const int drb = 8 * (xcd >> 1) + (r >> 2), dcb = 4 * (xcd & 1) + (r & 3);
    const int rc = 8 * (lane & 7) + 4 * ((lane >> 3) & 1) + 2 * ((lane >> 4) & 1) + (lane >> 5);
    const rsrc_t rs = __builtin_amdgcn_make_buffer_rsrc((void*)ws, 0, WS_USED, 0x00020000);
    const v4u rd = {kb[2], kb[3] & 0xffffu, WS_USED, 0x00020000u};

    unsigned long long pc0; int cend0, cend1;
    asm volatile("s_getpc_b64 %0\n.Lfs_begin:\n\ts_mov_b32 %1, .Lfs_end0-.Lfs_begin\n\ts_mov_b32 %2, .Lfs_end1-.Lfs_begin" : "=s"(pc0), "=s"(cend0), "=s"(cend1));
    auto code_warm = [&]() __attribute__((always_inline)) {
        const int o = (r + 32 * lane) * 128;
        unsigned t = 0;
        if (o < max(cend0, cend1)) t = *(const GAS unsigned*)(pc0 + o);
        return t;
    };

    if (wv >= NAUX) {
        const int w = wv - NAUX;
        const unsigned base = (unsigned)(g * SK + sp * (TOKIL ? 16 : 1024) + w * 4) * (DH * 2);
        const rsrc_t rk = __builtin_amdgcn_make_buffer_rsrc((void*)kp(1), 0, 8u * SK * DH * 2, 0x00020000);
        const rsrc_t rv = __builtin_amdgcn_make_buffer_rsrc((void*)kp(2), 0, 8u * SK * DH * 2, 0x00020000);
        const unsigned ko = base + ((lane >> 2) & 3) * 256 + (lane >> 4) * 64 + (lane & 3) * 16, vo = base + lane * 16;
#define ROT 0
        const unsigned rot = (ROT == 1 ? (b >> 2) & 63 : ROT == 2 ? (b & 63) : ROT == 3 ? ((b >> 2) & 48) : ROT == 4 ? ((b * 16 + (b >> 2)) & 63) : 0) * CSTR;
        const int qp = lane & 3;
        const bool o1 = qp & 1, o2 = qp & 2;
        const unsigned mk0 = qp == 0 ? 0xffffu : qp == 1 ? 0xffff0000u : 0u;
        const unsigned mk1 = qp == 2 ? 0xffffu : qp == 3 ? 0xffff0000u : 0u;
        v4u win[WIN]; v4u qb; float S[CH / 4]; unsigned pp[CH / 4];
        v4f acc0 = {0, 0, 0, 0}, acc1 = {0, 0, 0, 0};
        constexpr int VS = CH % WIN;
        static_assert(VS + DEP <= WIN && WIN + CH / 4 - 1 <= CH - 1 + DEP, "schedule");
        sfor<0, CH + DEP>([&](auto mi) __attribute__((always_inline)) {
            constexpr int m = decltype(mi)::value;
            if constexpr (m == BARPOS) wg_barrier();
            if constexpr (m >= WIN) wait_vm<DEPV - 1>(); else if constexpr (m >= DEP0) wait_vm<DEP0 - 1>();
            if constexpr (m == QSIG) { if (w == 0) lds_signal(&L.fk); }
            if constexpr (m == WIN) {
                STAMP(3);
                lds_wait(&L.fq, NAUX);
                qb = *(const v4u*)&L.q[((lane >> 2) & 3) * 64 + 16 * (lane >> 4) + 4 * qp];
            }
            sfor<0, CH / 4>([&](auto gii) __attribute__((always_inline)) {
                constexpr int gi = decltype(gii)::value;
                if constexpr (m == cmax(4 * gi + 3 + DEP, WIN + gi)) {
                    float e[4];
#pragma unroll
                    for (int c = 0; c < 4; ++c) {
                        v4f d = __builtin_amdgcn_mfma_f32_16x16x32_bf16(__builtin_bit_cast(v8bf, win[(4 * gi + c) % WIN]), __builtin_bit_cast(v8bf, qb), (v4f){0, 0, 0, 0}, 0, 0, 0);
                        e[c] = o2 ? (o1 ? d[3] : d[2]) : (o1 ? d[1] : d[0]);
                    }
                    const float k01 = (o1 ? e[1] : e[0]) + dppf<X1>(o1 ? e[0] : e[1]);
                    const float k23 = (o1 ? e[3] : e[2]) + dppf<X1>(o1 ? e[2] : e[3]);
                    S[gi] = rbf(rbf((o2 ? k23 : k01) + dppf<X2>(o2 ? k01 : k23)) * SCALE);
                }
            });
            __builtin_amdgcn_sched_barrier(0);
            if constexpr (m < CH) win[m % WIN] = __builtin_amdgcn_raw_buffer_load_b128(rk, ko, (m * CSTR) ^ rot, KV_AUX);
            else win[m % WIN] = __builtin_amdgcn_raw_buffer_load_b128(rv, vo, ((m - CH) * CSTR) ^ rot, KV_AUX);
            __builtin_amdgcn_sched_barrier(0);
        });
        {
            float mx = S[0];
#pragma unroll
            for (int i = 1; i < CH / 4; ++i) mx = fmaxf(mx, S[i]);
            mx = fmaxf(mx, dppf<X1>(mx)); mx = fmaxf(mx, dppf<X2>(mx)); mx = mx32(mx16(mx));
            const float mc = mx * LOG2E;
            float l = 0;
#pragma unroll
            for (int i = 0; i < CH / 4; ++i) { const float p = __builtin_amdgcn_exp2f(fmaf(S[i], LOG2E, -mc)); l += p; pp[i] = cvt_pk_bf16(p, p); }
            l += dppf<X1>(l); l += dppf<X2>(l); l = rs16(l, l); l = rs32(l, l);
            if (lane < 16 && qp == 0) { L.sm[w][lane >> 2] = mc; L.sl[w][lane >> 2] = l; }
            lds_signal(&L.cml);
            STAMP(4);
        }
        auto pv = [&](auto ji) __attribute__((always_inline)) {
            constexpr int j = decltype(ji)::value;
            const unsigned pb = dppu<(j & 3) * 0x55>(pp[j >> 2]);
            const v2u a = {pb & mk0, pb & mk1};
            const v4u v = win[VS + j];
            const v2u vlo = {v[0], v[1]}, vhi = {v[2], v[3]};
            acc0 = __builtin_amdgcn_mfma_f32_16x16x16bf16_1k(__builtin_bit_cast(v4s, a), __builtin_bit_cast(v4s, vlo), acc0, 0, 0, 0);
            acc1 = __builtin_amdgcn_mfma_f32_16x16x16bf16_1k(__builtin_bit_cast(v4s, a), __builtin_bit_cast(v4s, vhi), acc1, 0, 0, 0);
        };
#pragma nounroll
        for (int it = 1; it < CH / DEP; ++it) {
            sfor<0, DEP>([&](auto ji) __attribute__((always_inline)) {
                constexpr int j = decltype(ji)::value;
                pv(ji);
                if constexpr (DEPV < DEP) wait_vm<DEPV - 1>();
                __builtin_amdgcn_sched_barrier(0);
                win[VS + j] = __builtin_amdgcn_raw_buffer_load_b128(rv, vo, ((it * DEP + j) * CSTR) ^ rot, KV_AUX);
                __builtin_amdgcn_sched_barrier(0);
            });
#pragma unroll
            for (int i = 0; i + DEP / 4 < CH / 4; ++i) pp[i] = pp[i + DEP / 4];
            if (it == ML_IT) lds_signal(&L.fm);
            if (it == WO_IT) lds_signal(&L.fwo);
        }
        lds_wait(&L.cml, NSTR);
        const int h = lane >> 4;
        const float f = __builtin_amdgcn_exp2f(L.sm[w][h] - fmaxf(fmaxf(L.sm[0][h], L.sm[1][h]), fmaxf(L.sm[2][h], L.sm[3][h])));
        sfor<0, DEP>(pv);
        *(v4f*)&L.sacc[w][4 * lane] = acc0 * f;
        *(v4f*)&L.sacc[w][288 + 4 * lane] = acc1 * f;
        STAMP(6);
        lds_signal(&L.cs);
        v4u wdv[7];
        if (w == NSTR - 1) {
            gptr p = wd + (56 * drb + (lane >> 3)) * (D * 2) + dcb * 128 + (lane & 7) * 16;
#pragma unroll
            for (int k = 0; k < 7; ++k) wdv[k] = ldw(p + k * (8 * D * 2));
        }
        __builtin_amdgcn_sched_barrier(0);
        lds_wait(&L.cs, NSTR);
        {
            const int t = w * 64 + lane;
            const int idx = ((t >> 1) & 1) * 288 + (t >> 2) * 4 + (t & 1) * 2;
            v2f s = *(const v2f*)&L.sacc[0][idx];
#pragma unroll
            for (int i = 1; i < NSTR; ++i) s += *(const v2f*)&L.sacc[i][idx];
            put(rs, OFF_O + (g * 32 + sp) * 4096 + t * 16, __float_as_uint(s[0]), __float_as_uint(s[1]), L.tag[0], L.tag[1]);
            STAMP(7);
        }
        if (w == NSTR - 1) {
            lds_wait(&L.fh, 1);
            float hr[7];
#pragma unroll
            for (int k = 0; k < 7; ++k) hr[k] = L.hv[8 * k + (lane >> 3)];
            float u0, u1; gemv64<7>(wdv, hr, lane, u0, u1);
            u0 = rs32(u0, u0); u1 = rs32(u1, u1);
            const int pi = rc >> 1;
            if (lane < 32) put(rs, OFF_D + (((4 * dcb + (pi >> 3)) * 32 + drb) * 8 + (pi & 7)) * 16, __float_as_uint(u0), __float_as_uint(u1), L.tag[0], L.tag[1]);
        }
        if (w == 0) FLUSH(0x80d8);
        asm volatile(".Lfs_end0:");
        return;
    }

    const int a = wv;
    STAMP(0);
    unsigned cw = 0;
    if (a == 1) cw = code_warm();
    v2u ev = {0, 0};
    if (a == 2) ev = __builtin_amdgcn_raw_buffer_load_b64(rs, OFF_E, 0, 16);
    const v4u xv = *(const GAS v4u*)(x + lane * 16);
    const int qs = xcd * 32 + r, rbk = r & 3;
    const int row = 128 * rbk + 32 * a + (lane >> 3);
    v4u wqv[4], wfv[4], wuv[4];
    {
        gptr p = wq + row * (QN * 2) + (qs >> 2) * 128 + (lane & 7) * 16;
#pragma unroll
        for (int k = 0; k < 4; ++k) wqv[k] = ldw(p + k * (8 * QN * 2));
    }
#define XOFF 1
    const int gc = 2 * (((xcd >> 1) + XOFF) & 3) + (r >> 4), rb = r & 15, chh = xcd & 1;
    const bool fin = b < 32;
    const int fc = 16 * (chh ^ 1) + 4 * (xcd >> 1) + (r & 3);
    if (threadIdx.x == 0) { L.cq = 0; L.cf = 0; L.cml = 0; L.fq = 0; L.fwo = 0; L.cs = 0; L.fw = 0; L.fk = 0; L.st1 = 0; L.fm = 0; L.fh = 0; }
    __builtin_amdgcn_sched_barrier(0);
    wg_barrier();
    const bool ffn = r < 28 && !(xcd & 1);
    const int fu = (xcd >> 1) * 7 + (r >> 2);
    float xr[4];
    {
        float xf[8];
#pragma unroll
        for (int i = 0; i < 4; ++i) { xf[2 * i] = bf_lo(xv[i]); xf[2 * i + 1] = bf_hi(xv[i]); }
        float s = 0;
#pragma unroll
        for (int i = 0; i < 8; ++i) s += xf[i];
        const float mean = wave_sum(s) * (1.f / D);
        float q2 = 0;
#pragma unroll
        for (int i = 0; i < 8; ++i) { const float d = xf[i] - mean; q2 = fmaf(d, d, q2); }
        const float var = wave_sum(q2) * (1.f / D);
        const float mb = rbf(mean), sd = rbf(sqrtf(rbf(rbf(var) + 1e-5f)));
        *(v4f*)&L.xn[a][8 * lane] = (v4f){xf[0], xf[1], xf[2], xf[3]}; *(v4f*)&L.xn[a][8 * lane + 4] = (v4f){xf[4], xf[5], xf[6], xf[7]};
        cbar();
#pragma unroll
        for (int k = 0; k < 4; ++k) xr[k] = rbf(rbf(L.xn[a][row + 8 * k] - mb) / sd);
    }
    {
        float u0, u1; gemv64<4>(wqv, xr, lane, u0, u1);
        L.redq[a][rc] = rs32(u0, u1);
        if (a == 2 && lane == 0) { L.tag[0] = ev[0] + 1u; L.tag[1] = ev[1] + 1u; }
        lds_signal(&L.cq);
        asm volatile("" :: "v"(cw));
    }
    if (a == 0) {
        lds_wait(&L.cq, NAUX);
        if (lane < 32) {
            const unsigned ta = L.tag[0], tb = L.tag[1];
            const v2f s = *(const v2f*)&L.redq[0][2 * lane] + *(const v2f*)&L.redq[1][2 * lane] + *(const v2f*)&L.redq[2][2 * lane] + *(const v2f*)&L.redq[3][2 * lane];
            put(rs, OFF_Q + (qs * 32 + lane) * 16, __float_as_uint(s[0]), __float_as_uint(s[1]), ta, tb);
        }
        STAMP(1);
    }
    __builtin_amdgcn_sched_barrier(0);
    if (ffn) {
        const int o = row * (DF * 2) + fu * 128 + (lane & 7) * 16;
#pragma unroll
        for (int k = 0; k < 4; ++k) wfv[k] = ldw(wg + o + k * (8 * DF * 2));
#pragma unroll
        for (int k = 0; k < 4; ++k) wuv[k] = ldw(wu + o + k * (8 * DF * 2));
    }
    __builtin_amdgcn_sched_barrier(0);
    if (ffn) {
        float u0, u1; gemv64<4>(wfv, xr, lane, u0, u1);
        L.redf[a][rc] = rs32(u0, u1);
        gemv64<4>(wuv, xr, lane, u0, u1);
        L.redf[a][64 + rc] = rs32(u0, u1);
        lds_signal(&L.cf);
    }
    lds_wait(&L.cq, NAUX);
    const unsigned ta = __builtin_amdgcn_readfirstlane(L.tag[0]), tb = __builtin_amdgcn_readfirstlane(L.tag[1]);
    if (a == 1 && ffn) {
        lds_wait(&L.cf, NAUX);
        const v2f s = *(const v2f*)&L.redf[0][2 * lane] + *(const v2f*)&L.redf[1][2 * lane] + *(const v2f*)&L.redf[2][2 * lane] + *(const v2f*)&L.redf[3][2 * lane];
        put(rs, OFF_H + (((2 * fu + (lane >> 5)) * 4 + rbk) * 32 + (lane & 31)) * 16, __float_as_uint(s[0]), __float_as_uint(s[1]), ta, tb);
    }
    G4 A = {};
    {
        lds_wait(&L.fk, 1);
        gather4<SLP_Q>(rd, OFF_Q + ((8 * g + 2 * a + (lane >> 5)) * 128 + (lane & 31)) * 16, 512, A, ta, tb);
        const float q0 = (__uint_as_float(A.g[0][0]) + __uint_as_float(A.g[1][0])) + (__uint_as_float(A.g[2][0]) + __uint_as_float(A.g[3][0]));
        const float q1 = (__uint_as_float(A.g[0][2]) + __uint_as_float(A.g[1][2])) + (__uint_as_float(A.g[2][2]) + __uint_as_float(A.g[3][2]));
        L.q[64 * a + lane] = cvt_pk_bf16(q0, q1);
        lds_signal(&L.fq);
        STAMP(2);
    }
    float xo = 0;
    if (a == 3) {
        if (fin) xo = bf_lo(*(const GAS unsigned short*)(x + fc * 32 + (lane & 15) * 2));
        const int l = lane < 56 ? lane : 55, mat = l >= 28, c = 56 * drb + 2 * (l - 28 * mat);
        lds_wait(&L.cml, NSTR);
        gather4<SLP_F>(rd, OFF_H + (((c >> 6) * 2 + mat) * 128 + ((c & 63) >> 1)) * 16, 512, A, ta, tb);
        const float s0 = (__uint_as_float(A.g[0][0]) + __uint_as_float(A.g[1][0])) + (__uint_as_float(A.g[2][0]) + __uint_as_float(A.g[3][0]));
        const float s1 = (__uint_as_float(A.g[0][2]) + __uint_as_float(A.g[1][2])) + (__uint_as_float(A.g[2][2]) + __uint_as_float(A.g[3][2]));
        *(v2f*)&L.hs[mat][2 * (l - 28 * mat)] = (v2f){s0, s1};
        cbar();
        {
            const float gg = rbf(L.hs[0][l]), uu = rbf(L.hs[1][l]);
            const float th = 1.f - 2.f / (__expf(1.5957691216057308f * (gg + 0.044715f * gg * gg * gg)) + 1.f);
            L.hv[lane] = rbf(rbf(0.5f * gg * (1.f + th)) * uu);
        }
        lds_signal(&L.fh);
    }
    if (a == 2) {
        lds_wait(&L.cml, NSTR);
        const float m = L.sm[lane & 3][(lane >> 2) & 3], l = L.sl[lane & 3][(lane >> 2) & 3];
        float M = fmaxf(m, dppf<X1>(m)); M = fmaxf(M, dppf<X2>(M));
        float s = l * __builtin_amdgcn_exp2f(m - M);
        s += dppf<X1>(s); s += dppf<X2>(s);
        if (lane < 16 && (lane & 3) == 0) put(rs, OFF_ML + ((g * 4 + (lane >> 2)) * 32 + sp) * 16, __float_as_uint(M), __float_as_uint(s), ta, tb);
    }
    lds_wait(&L.fm, 1);
    if (a == 1) cw = code_warm();
    if (a == 2) {
        v4u gm;
        gather1<SLP_F>(rd, OFF_ML + ((gc * 4 + (rb >> 2)) * 32 + (lane & 31)) * 16, gm, ta, tb);
        const float m0 = __uint_as_float(gm[0]);
        float G = fmaxf(m0, dppf<X1>(m0)); G = fmaxf(G, dppf<X2>(G)); G = fmaxf(G, dppf<ROR4>(G)); G = fmaxf(G, dppf<ROR8>(G)); G = mx16(G);
        const float e = __builtin_amdgcn_exp2f(m0 - G);
        float T = __uint_as_float(gm[2]) * e;
        T += dppf<X1>(T); T += dppf<X2>(T); T += dppf<ROR4>(T); T += dppf<ROR8>(T); T = rs16(T, T);
        if (lane < 32) L.wgt[lane] = e / T;
        lds_signal(&L.fw);
        STAMP(5);
    }
    v16u WT;
    {
        asm volatile("" :: "v"(cw));
        lds_wait(WO_IT < 4 ? &L.fwo : &L.cs, WO_N);
        gptr p = wo + (512 * gc + 32 * rb + 8 * a + (lane >> 5)) * (D * 2) + chh * 512 + (lane & 31) * 16;
        asm volatile("global_load_dwordx4 v[184:187], %1, off nt\n\tglobal_load_dwordx4 v[188:191], %1, off offset:2048 nt\n\t"
                     "global_load_dwordx4 v[192:195], %2, off nt\n\tglobal_load_dwordx4 v[196:199], %2, off offset:2048 nt"
                     : "={v[184:199]}"(WT) : "v"(p), "v"(p + 4096) : "memory");
    }
    lds_wait(&L.fw, 1);
    lds_wait(&L.cs, NSTR);
    STAMP(13);
    v16u PA_, PB_, PC_;
    {
        const float wa = L.wgt[lane >> 2], wb = L.wgt[16 + (lane >> 2)];
        float v0, v1;
        gatherp2(rd, OFF_O + (gc * 32 + (lane >> 2)) * 4096 + (16 * rb + 4 * a + (lane & 3)) * 16, 16 * 4096, PA_, PB_, PC_, WT, wa, wb, v0, v1, ta, tb);
        STAMP(8);
        v0 += dppf<ROR4>(v0); v1 += dppf<ROR4>(v1); v0 += dppf<ROR8>(v0); v1 += dppf<ROR8>(v1);
        const float o = rbf(rs32(rs16(v0, v0), rs16(v1, v1)));
        const float ok[4] = {dppf<0x00>(o), dppf<0x55>(o), dppf<0xAA>(o), dppf<0xFF>(o)};
        float c[8];
#pragma unroll
        for (int i = 0; i < 4; ++i) {
            c[2 * i] = ok[0] * bf_lo(WT[i]) + ok[1] * bf_lo(WT[4 + i]) + ok[2] * bf_lo(WT[8 + i]) + ok[3] * bf_lo(WT[12 + i]);
            c[2 * i + 1] = ok[0] * bf_hi(WT[i]) + ok[1] * bf_hi(WT[4 + i]) + ok[2] * bf_hi(WT[8 + i]) + ok[3] * bf_hi(WT[12 + i]);
        }
        v4f s4;
#pragma unroll
        for (int i = 0; i < 4; ++i) s4[i] = rs32(c[i], c[4 + i]);
        *(v4f*)&L.ss[a][8 * (lane & 31) + 4 * (lane >> 5)] = s4;
    }
    wg_barrier();
    if (a < 2) {
        const int n = 64 * a + lane;
        const v2f s = *(const v2f*)&L.ss[0][2 * n] + *(const v2f*)&L.ss[1][2 * n] + *(const v2f*)&L.ss[2][2 * n] + *(const v2f*)&L.ss[3][2 * n];
        put(rs, OFF_S + (((16 * chh + (n >> 3)) * 128 + gc * 16 + rb) * 8 + (n & 7)) * 16, __float_as_uint(s[0]), __float_as_uint(s[1]), ta, tb);
        STAMP(9);
    }
    if (fin) {
        float v0, v1, d0, d1;
        gatherp5(rd, OFF_S + (fc * 1024 + a * 256 + lane) * 16, OFF_D + (fc * 256 + a * 64 + lane) * 16, PA_, PB_, PC_, WT, v0, v1, d0, d1, ta, tb);
        STAMP(10);
        v0 += dppf<ROR8>(v0); v1 += dppf<ROR8>(v1);
        float v = rs16(v0, v1); v = rs32(v, v);
        d0 += dppf<ROR8>(d0); d1 += dppf<ROR8>(d1);
        float d = rs16(d0, d1); d = rs32(d, d);
        if ((lane & 40) == 0) { L.fin[a][2 * (lane & 7) + (lane >> 4)] = v; L.ds[a][2 * (lane & 7) + (lane >> 4)] = d; }
        wg_barrier();
    }
    if (a == 3) {
        if (fin) {
            const int c = lane & 15;
            const float s = rbf((L.fin[0][c] + L.fin[1][c]) + (L.fin[2][c] + L.fin[3][c]));
            const float dd = rbf((L.ds[0][c] + L.ds[1][c]) + (L.ds[2][c] + L.ds[3][c]));
            const float res = xo + rbf(s * rbf(dd / (1.f + __expf(-dd))));
            if (lane < 16) *(GAS unsigned short*)(out + fc * 32 + lane * 2) = (unsigned short)(cvt_pk_bf16(res, res) & 0xffffu);
            if (b == 0 && lane == 0) __builtin_amdgcn_raw_buffer_store_b64((v2u){ta, tb}, rs, OFF_E, 0, 16);
        }
        STAMP(11);
    }
    asm volatile("" :: "{v[200:215]}"(PA_), "{v[216:231]}"(PB_), "{v[232:247]}"(PC_), "{v[184:199]}"(WT));
    if (a == 0) FLUSH(0x6307);
    if (a == 2) FLUSH(0x20);
    if (a == 3) FLUSH(0x1c00);
    asm volatile(".Lfs_end1:");
}

extern "C" void kernel_launch(void* const* d_in, const int* in_sizes, int n_in, void* d_out, int out_size, void* d_ws, size_t ws_size, hipStream_t stream) {
    fused_step<<<256, NTHR, 0, stream>>>(d_in[0], d_in[1], d_in[2], d_in[3], d_in[4], d_in[6], d_in[7], d_in[8], d_out, d_ws);
}
```
